```python
import torch

def _softmax(x, axis=-1):
    return torch.softmax(x, dim=axis)
def _gelu(x):
    return torch.nn.functional.gelu(x, approximate="tanh")
def _silu(x):
    return torch.nn.functional.silu(x)
def _sigmoid(x):
    return torch.sigmoid(x)
def _layernorm(x, axis=-1, eps=1e-5):
    m = x.mean(dim=axis, keepdim=True)
    v = x.var(dim=axis, keepdim=True, unbiased=False)
    return (x - m) / torch.sqrt(v + eps)
def _scatter_add(tgt, idx, src, axis=0):
    return tgt.clone().index_add_(axis, idx, src)
def _segment_sum(src, seg, n):
    out = torch.zeros((n,) + src.shape[1:], dtype=src.dtype)
    return out.index_add_(0, seg, src)
def _pad(x, pw):
    flat = tuple(v for pair in reversed(pw) for v in pair)
    return torch.nn.functional.pad(x, flat)

kernel_name = 'parallel_sum__sliding_local_dilated__glu_gated_glu__prompt'

def setup_inputs():

    _w = (lambda a: (lambda t: t.to(torch.bfloat16) if t.is_floating_point() else t)(torch.as_tensor(a)))
    def _A_setup():
        B, S, D = 1, 2048, 640
        return {
            "x": _w((lambda *s: torch.randn(*s, dtype=torch.bfloat16) * 0.05)(B, S, D)),
            "wqkv": _w((lambda *s: torch.randn(*s, dtype=torch.bfloat16) * 0.05)(D, 3 * D)),
            "wo": _w((lambda *s: torch.randn(*s, dtype=torch.bfloat16) * 0.05)(D, D)),
        }
    _A_in = _A_setup()

    def _B_setup():
        B, S, D, F = 1, 2048, 640, 1536
        return {
            "x": _w((lambda *s: torch.randn(*s, dtype=torch.bfloat16) * 0.05)(B, S, D)),
            "w_gate": _w((lambda *s: torch.randn(*s, dtype=torch.bfloat16) * 0.05)(D, F)),
            "w_up": _w((lambda *s: torch.randn(*s, dtype=torch.bfloat16) * 0.05)(D, F)),
            "w_down": _w((lambda *s: torch.randn(*s, dtype=torch.bfloat16) * 0.05)(F, D)),
        }
    _B_in = _B_setup()

    return {
        "A_x": _A_in["x"],
        "A_wqkv": _A_in["wqkv"],
        "A_wo": _A_in["wo"],
        "B_x": _B_in["x"],
        "B_w_gate": _B_in["w_gate"],
        "B_w_up": _B_in["w_up"],
        "B_w_down": _B_in["w_down"],
    }

def reference(A_x, A_wqkv, A_wo, B_x, B_w_gate, B_w_up, B_w_down):
    def _A_ref(x, wqkv, wo):
        B, S, D = x.shape
        H = 32; Dh = D // H
        W, dil = 8, 2
        qkv = (lambda x, *d: x.permute(*d))((x @ wqkv).reshape(B, S, 3, H, Dh), 2, 0, 3, 1, 4)
        q, k, v = qkv[0], qkv[1], qkv[2]
        scale = Dh ** -0.5
        pos = torch.arange(S)
        off = torch.arange(W) * dil
        idx = pos[:, None] - off[None, :]
        mask = idx >= 0
        idx_c = torch.where(mask, idx, 0).reshape(-1)
        kg = k[:, :, idx_c, :].reshape(B, H, S, W, Dh)
        vg = v[:, :, idx_c, :].reshape(B, H, S, W, Dh)
        logits = torch.einsum("bhsd,bhswd->bhsw", q, kg) * scale
        logits = torch.where(mask[None, None, :, :], logits, -1e9)
        attn = _softmax(logits, axis=-1)
        o = torch.einsum("bhsw,bhswd->bhsd", attn, vg)
        return (lambda x, *d: x.permute(*d))(o, 0, 2, 1, 3).reshape(B, S, D) @ wo

    def _B_ref(x, w_gate, w_up, w_down):
        g = torch.einsum("bsd,df->bsf", x, w_gate)
        u = torch.einsum("bsd,df->bsf", x, w_up)
        h = _sigmoid(g) * u
        return torch.einsum("bsf,fd->bsd", h, w_down)

    x = A_x
    xn = _layernorm(x, axis=-1)
    return x + _A_ref(xn, A_wqkv, A_wo) + _B_ref(xn, B_w_gate, B_w_up, B_w_down)
```

```cpp
#include <hip/hip_runtime.h>
#include <hip/hip_bf16.h>
#include <type_traits>

typedef short s16x4 __attribute__((ext_vector_type(4)));
typedef short s16x8 __attribute__((ext_vector_type(8)));
typedef __bf16 bf16x2 __attribute__((ext_vector_type(2)));
typedef __bf16 bf16x8 __attribute__((ext_vector_type(8)));
typedef float f32x2 __attribute__((ext_vector_type(2)));
typedef float f32x4 __attribute__((ext_vector_type(4)));
typedef unsigned u32x2 __attribute__((ext_vector_type(2)));
typedef unsigned u32x4 __attribute__((ext_vector_type(4)));
#define LDSP(T, a) ((__attribute__((address_space(3))) T*)(size_t)(unsigned)(a))

constexpr int S_ = 2048, D_ = 640, F_ = 1536;
constexpr int NKS2 = 68;
constexpr size_t WS_A2 = 0;
constexpr size_t WS_W2 = 9437184;

constexpr int A_SLOT = 22528;
constexpr int LDS_A = 0;
constexpr int B_SLOT = 16384;
constexpr int LDS_B = 3 * A_SLOT;
constexpr int LDS_ST = LDS_B + 5 * B_SLOT;
constexpr int LDS_CS = LDS_ST + 4 * 176 * 8;
constexpr int LDS_K1 = LDS_CS + 1024;
constexpr int QROW = 488;

__device__ __forceinline__ void dma16(const void* sbase, unsigned voff, unsigned lds_addr) {
  unsigned keep;
  asm volatile("s_mov_b32 %0, m0\n\ts_mov_b32 m0, %3\n\ts_nop 0\n\tglobal_load_lds_dwordx4 %1, %2\n\ts_mov_b32 m0, %0"
               : "=&s"(keep) : "v"(voff), "s"(sbase), "s"(lds_addr) : "memory");
}
template <int N> __device__ __forceinline__ void wait_vm() { asm volatile("s_waitcnt vmcnt(%0)" ::"i"(N) : "memory"); }
__device__ __forceinline__ void wait_lgkm0() { asm volatile("s_waitcnt lgkmcnt(0)" ::: "memory"); }

__device__ __forceinline__ bf16x8 tr_frag(unsigned a0, unsigned a1) {
  s16x4 lo = __builtin_amdgcn_ds_read_tr16_b64_v4i16(LDSP(s16x4, a0));
  s16x4 hi = __builtin_amdgcn_ds_read_tr16_b64_v4i16(LDSP(s16x4, a1));
  s16x8 v = __builtin_shufflevector(lo, hi, 0, 1, 2, 3, 4, 5, 6, 7);
  return __builtin_bit_cast(bf16x8, v);
}
__device__ __forceinline__ unsigned pack2(float a, float b) {
  bf16x2 p = {(__bf16)a, (__bf16)b};
  return __builtin_bit_cast(unsigned, p);
}

template <int I, int N, class Fn> __device__ __forceinline__ void static_for(Fn&& f) {
  if constexpr (I < N) { f(std::integral_constant<int, I>{}); static_for<I + 1, N>(f); }
}

constexpr int n_issue(int s, int a) {
  int n = 0;
  if (s + 5 <= 19) n += 2;
  if ((s & 1) && ((s - 1) / 2 + 3) <= 10) n += a;
  return n;
}
constexpr int n_allowed(int t, int a) { return n_issue(t - 3, a) + n_issue(t - 2, a) + n_issue(t - 1, a); }

template <int mh>
__device__ __forceinline__ void k1_body(const __hip_bfloat16* __restrict__ x,
                                        const __hip_bfloat16* __restrict__ wqkv,
                                        const __hip_bfloat16* __restrict__ wo,
                                        const __hip_bfloat16* __restrict__ wg,
                                        const __hip_bfloat16* __restrict__ wu,
                                        const __hip_bfloat16* __restrict__ wd,
                                        char* __restrict__ ws, const int wave) {
  constexpr int NM = mh == 0 ? 6 : 5;
  const int tid = threadIdx.x, lane = tid & 63;
  const int wq = wave & 3;
  const int q = lane >> 4, c = lane & 15;

  const int xcd = blockIdx.x & 7, idx = blockIdx.x >> 3;
  const int xr = xcd >> 2, xc = xcd & 3;
  const int natt = xr == 0 ? 14 : 12;
  const bool is_att = idx < natt;
  int tok0, cbk = 0, hg = 0, job = 1000;
  if (is_att) {
    const int ab = (xr == 0 ? 0 : 7) + (idx >> 1);
    hg = 2 * xc + (idx & 1);
    tok0 = 160 * ab - 16;
  } else {
    const int j = idx - natt;
    if (j >= 18) return;
    const int rb = 6 * xr + j / 3;
    cbk = 3 * xc + j % 3;
    tok0 = 175 * rb - 1;
    job = rb * 12 + cbk;
  }

  unsigned a_voff[3];
#pragma unroll
  for (int e = 0; e < 3; ++e) {
    const int ii = (wave + 8 * e) < 22 ? wave + 8 * e : wave + 8;
    const int row = 8 * ii + (lane >> 3);
    int tok = tok0 + row;
    tok = tok < 0 ? 0 : (tok > S_ - 1 ? S_ - 1 : tok);
    a_voff[e] = (unsigned)(tok * 1280 + (((lane & 7) ^ ((row >> 1) & 7)) << 4));
  }
  unsigned b_voff[2];
  const char* b_base[2];
  int b_step;
  {
    const int row = 4 * wave + (lane >> 4);
    const int hsw = (row & 3) | (((row >> 3) & 1) << 2);
    const int g = (lane & 15) ^ (hsw << 1);
    if (is_att) {
#pragma unroll
      for (int e = 0; e < 2; ++e) {
        int L = 16 * e + g;
        L = L > 29 ? 29 : L;
        const int sec = L / 10;
        b_voff[e] = (unsigned)(row * 3840 + (640 * sec + 80 * hg + 8 * (L - 10 * sec)) * 2);
      }
      b_base[0] = (const char*)wqkv; b_base[1] = (const char*)wqkv;
      b_step = 32 * 3840;
    } else {
      b_voff[0] = b_voff[1] = (unsigned)(row * 3072 + (128 * cbk + 8 * g) * 2);
      b_base[0] = (const char*)wg; b_base[1] = (const char*)wu;
      b_step = 32 * 3072;
    }
  }
  const unsigned a_dst2 = wave < 6 ? 16384u : 8192u;

  auto issueB = [&](int j, unsigned slot_off) {
#pragma unroll
    for (int e = 0; e < 2; ++e)
      dma16(b_base[e] + (size_t)j * b_step, b_voff[e], LDS_B + slot_off + e * 8192 + wave * 1024);
  };
  auto issueA = [&](int j, unsigned slot_off) {
    const char* ab = (const char*)x + j * 128;
    const unsigned dst = LDS_A + slot_off + wave * 1024;
    if (wave != 0 || lane >= 8) dma16(ab, a_voff[0], dst);
    dma16(ab, a_voff[1], dst + 8192);
    dma16(ab, a_voff[2], dst + a_dst2);
  };
  auto issueJob = [&]() {
    const int jb = job < 136 ? job : 0;
    const int ks = jb >> 1, half = jb & 1;
    const char* wb = ks < 20 ? (const char*)wo + (size_t)ks * 32 * 1280 : (const char*)wd + (size_t)(ks - 20) * 32 * 1280;
    const unsigned dst = LDS_A + 1 * A_SLOT + wave * 1024;
#pragma unroll
    for (int e = 0; e < 3; ++e) {
      const int pe = (wave + 8 * e) < 20 ? e : 1;
      const int o = 1024 * (wave + 8 * pe) + 16 * lane;
      const int row = o / 640;
      dma16(wb, (unsigned)(row * 1280 + 640 * half + (o - row * 640)), dst + pe * 8192);
    }
  };

  issueB(0, 0); issueA(0, 0); issueB(1, B_SLOT); issueB(2, 2 * B_SLOT); issueA(1, A_SLOT);
  issueB(3, 3 * B_SLOT); issueB(4, 4 * B_SLOT); issueA(2, 2 * A_SLOT);
  if (tid < 24) {
    u32x4 ones = {0x3F803F80u, 0x3F803F80u, 0x3F803F80u, 0x3F803F80u};
    *LDSP(u32x4, LDS_A + (tid >> 3) * A_SLOT + (tid & 7) * 16) = ones;
  }

  unsigned a_rd[2];
#pragma unroll
  for (int s = 0; s < 2; ++s)
    a_rd[s] = LDS_A + mh * 6 * 2048 + c * 128 + (((4 * s + q) ^ ((c >> 1) & 7)) << 4);
  unsigned b_rd[4];
  int ntile[4];
  {
    const int a = c >> 2, p = c & 3;
    const int hh = a | ((q & 1) << 2);
#pragma unroll
    for (int n = 0; n < 4; ++n) {
      const int nt = is_att ? 4 * wq + n : (n < 2 ? 2 * wq + n : 8 + 2 * wq + n - 2);
      ntile[n] = nt;
      b_rd[n] = LDS_B + (nt >> 3) * 8192 + (8 * q + a) * 256 + (((nt & 7) ^ hh) << 5) + 8 * p;
    }
  }

  f32x4 acc[NM][4];
#pragma unroll
  for (int m = 0; m < NM; ++m)
#pragma unroll
    for (int n = 0; n < 4; ++n) acc[m][n] = f32x4{0.f, 0.f, 0.f, 0.f};
  float ssum[NM], ssq[NM];
#pragma unroll
  for (int m = 0; m < NM; ++m) { ssum[m] = 0.f; ssq[m] = 0.f; }

  bf16x8 af[2][NM], bfr[2][4];
  auto load_frags = [&](auto par, unsigned ao, unsigned bo) {
    constexpr int sl = decltype(par)::value;
#pragma unroll
    for (int n = 0; n < 4; ++n) bfr[sl][n] = tr_frag(b_rd[n] + bo, b_rd[n] + bo + 1024);
#pragma unroll
    for (int m = 0; m < NM; ++m) af[sl][m] = *LDSP(bf16x8, a_rd[sl] + ao + m * 2048);
  };
  const bf16x2 one2 = {(__bf16)1.0f, (__bf16)1.0f};
  auto compute = [&](auto par, int t) {
    constexpr int sl = decltype(par)::value;
#pragma unroll
    for (int m = 0; m < NM; ++m)
#pragma unroll
      for (int n = 0; n < 4; ++n)
        asm volatile("v_mfma_f32_16x16x32_bf16 %0, %1, %2, %0" : "+a"(acc[m][n]) : "v"(bfr[sl][n]), "v"(af[sl][m]));
    if (((t + 2 * mh) & 3) == wq) {
#pragma unroll
      for (int m = 0; m < NM; ++m) {
#pragma unroll
        for (int j = 0; j < 4; ++j) {
          bf16x2 v = {af[sl][m][2 * j], af[sl][m][2 * j + 1]};
          ssum[m] = __builtin_amdgcn_fdot2_f32_bf16(v, one2, ssum[m], false);
          ssq[m] = __builtin_amdgcn_fdot2_f32_bf16(v, v, ssq[m], false);
        }
      }
    }
  };
  using P0 = std::integral_constant<int, 0>;
  using P1 = std::integral_constant<int, 1>;

#pragma unroll
  for (int m = 0; m < NM; ++m)
#pragma unroll
    for (int n = 0; n < 4; ++n) asm volatile("" : "+a"(acc[m][n]));
  wait_vm<n_allowed(-1, 3) + n_issue(-1, 3)>();
  wait_lgkm0();
  __builtin_amdgcn_s_barrier();
  load_frags(P0{}, 0, 0);

  unsigned bo = 0;
  unsigned ao = 0;
#pragma unroll 1
  for (int t = 0; t < 14; t += 2) {
    const unsigned bo1 = bo == 4 * B_SLOT ? 0 : bo + B_SLOT;
    const unsigned bo2 = bo1 == 4 * B_SLOT ? 0 : bo1 + B_SLOT;
    const unsigned ao1 = ao == 2 * A_SLOT ? 0 : ao + A_SLOT;
    wait_vm<12>();
    wait_lgkm0();
    __builtin_amdgcn_s_barrier();
    issueB(t + 5, bo);
    load_frags(P1{}, ao, bo1);
    compute(P0{}, t);
    wait_vm<9>();
    wait_lgkm0();
    __builtin_amdgcn_s_barrier();
    issueB(t + 6, bo1);
    issueA((t >> 1) + 3, ao);
    load_frags(P0{}, ao1, bo2);
    compute(P1{}, t + 1);
    bo = bo2; ao = ao1;
  }
  static_for<14, 20>([&](auto tc) {
    constexpr int t = decltype(tc)::value;
    wait_vm<n_allowed(t, 3)>();
    wait_lgkm0();
    __builtin_amdgcn_s_barrier();
    if (t == 14) issueB(19, (19 % 5) * B_SLOT);
    if (t == 15) issueJob();
    if (t < 19) load_frags(std::integral_constant<int, (t + 1) & 1>{}, (((t + 1) >> 1) % 3) * A_SLOT, ((t + 1) % 5) * B_SLOT);
    compute(std::integral_constant<int, t & 1>{}, t);
  });

  asm volatile("s_nop 15\n\ts_nop 15" ::: "memory");
#pragma unroll
  for (int m = 0; m < NM; ++m) {
    if (m < 5 || mh == 0) {
      float a = ssum[m], b = ssq[m];
      a += __shfl_xor(a, 16); b += __shfl_xor(b, 16);
      a += __shfl_xor(a, 32); b += __shfl_xor(b, 32);
      if (q == 0) *LDSP(f32x2, LDS_ST + (wq * 176 + (6 * mh + m) * 16 + c) * 8) = f32x2{a, b};
    }
  }
  if (mh == 0 && c == 0) {
#pragma unroll
    for (int n = 0; n < 4; ++n) *LDSP(f32x4, LDS_CS + (ntile[n] * 16 + 4 * q) * 4) = acc[0][n];
  }
  wait_vm<0>();
  wait_lgkm0();
  __builtin_amdgcn_s_barrier();

  f32x4 cs[4];
#pragma unroll
  for (int n = 0; n < 4; ++n) cs[n] = *LDSP(f32x4, LDS_CS + (ntile[n] * 16 + 4 * q) * 4);
  float mean[NM], rstd[NM];
#pragma unroll
  for (int m = 0; m < NM; ++m) {
    if (m < 5 || mh == 0) {
      float a = 0.f, b = 0.f;
#pragma unroll
      for (int w = 0; w < 4; ++w) {
        f32x2 v = *LDSP(f32x2, LDS_ST + (w * 176 + (6 * mh + m) * 16 + c) * 8);
        a += v[0]; b += v[1];
      }
      const float mu = a * (1.0f / D_);
      float var = b * (1.0f / D_) - mu * mu;
      var = var < 0.f ? 0.f : var;
      mean[m] = mu;
      rstd[m] = __builtin_amdgcn_rsqf(var + 1e-5f);
    }
  }

  char* a2 = ws + WS_A2;
  if (!is_att) {
    const int ks = 20 + 4 * cbk + wq;
#pragma unroll
    for (int m = 0; m < NM; ++m) {
      if (m < 5 || mh == 0) {
        const int trow = (6 * mh + m) * 16 + c;
        const int tok = tok0 + trow;
        float hv[8];
#pragma unroll
        for (int n = 0; n < 2; ++n)
#pragma unroll
          for (int r = 0; r < 4; ++r) {
            const float g = rstd[m] * (acc[m][n][r] - mean[m] * cs[n][r]);
            const float u = rstd[m] * (acc[m][n + 2][r] - mean[m] * cs[n + 2][r]);
            hv[4 * n + r] = u * __builtin_amdgcn_rcpf(1.0f + __expf(-g));
          }
        u32x4 pk = {pack2(hv[0], hv[1]), pack2(hv[2], hv[3]), pack2(hv[4], hv[5]), pack2(hv[6], hv[7])};
        if (trow >= 1 && tok < S_)
          *(u32x4*)(a2 + (size_t)(((tok >> 6) * NKS2 + ks) * 4 + ((tok >> 4) & 3)) * 1024 + (16 * q + (tok & 15)) * 16) = pk;
      }
    }
    if (job < 136) {
      const int ks2 = job >> 1, half = job & 1;
      const int a = c >> 2, p = c & 3;
#pragma unroll
      for (int e = 0; e < 3; ++e) {
        const int u = wave + 8 * e;
        if (u < 20) {
          const int n5 = u % 5, cl = u / 5;
          const int colst = n5 < 4 ? 32 * (n5 >> 1) + 8 * p + 4 * (n5 & 1) : 64 + 4 * p;
          const unsigned ad = LDS_A + 1 * A_SLOT + (4 * q + a) * 640 + (80 * cl + colst) * 2;
          bf16x8 f = tr_frag(ad, ad + 16 * 640);
          const int cb2 = 4 * half + cl;
          *(bf16x8*)(ws + WS_W2 + (size_t)((cb2 * NKS2 + ks2) * 5 + n5) * 1024 + lane * 16) = f;
        }
      }
    }
  } else {
#pragma unroll
    for (int m = 0; m < NM; ++m) {
      if (m < 5 || mh == 0) {
        const int trow = (6 * mh + m) * 16 + c;
#pragma unroll
        for (int n = 0; n < 4; ++n) {
          float y[4];
#pragma unroll
          for (int r = 0; r < 4; ++r) y[r] = rstd[m] * (acc[m][n][r] - mean[m] * cs[n][r]);
          if (ntile[n] < 15)
            *LDSP(u32x2, trow * QROW + (ntile[n] * 16 + 4 * q) * 2) = u32x2{pack2(y[0], y[1]), pack2(y[2], y[3])};
        }
      }
    }
    wait_lgkm0();
    __builtin_amdgcn_s_barrier();
    const float scale = 0.22360679774997896f;
    const int dofs = q < 2 ? 16 * q : 32;
    const int a = c >> 2, p = c & 3;
#pragma unroll
    for (int i = 0; i < 5; ++i) {
      const int unit = wave * 5 + i;
      const int mq = 1 + (unit >> 2), hh = unit & 3;
      const unsigned qa = (16 * mq + c) * QROW + 40 * hh + dofs;
      u32x2 q0 = *LDSP(u32x2, qa), q1 = *LDSP(u32x2, qa + 8);
      if (q >= 2) { q1 = u32x2{0u, 0u}; }
      if (q == 3) { q0 = u32x2{0u, 0u}; }
      const bf16x8 qf = __builtin_bit_cast(bf16x8, u32x4{q0[0], q0[1], q1[0], q1[1]});
      f32x4 sc[2];
#pragma unroll
      for (int kt = 0; kt < 2; ++kt) {
        const unsigned ka = (16 * (mq - 1 + kt) + c) * QROW + 160 + 40 * hh + dofs;
        u32x2 k0 = *LDSP(u32x2, ka), k1 = *LDSP(u32x2, ka + 8);
        const bf16x8 kf = __builtin_bit_cast(bf16x8, u32x4{k0[0], k0[1], k1[0], k1[1]});
        sc[kt] = __builtin_amdgcn_mfma_f32_16x16x32_bf16(kf, qf, f32x4{0.f, 0.f, 0.f, 0.f}, 0, 0, 0);
      }
      float lg[8];
      float mx = -3.0e38f;
#pragma unroll
      for (int j = 0; j < 8; ++j) {
        const int krel = 16 * (j >> 2) + 4 * q + (j & 3);
        const int diff = 16 + c - krel;
        const bool ok = diff >= 0 && diff <= 14 && (diff & 1) == 0 && (tok0 + 16 * (mq - 1) + krel) >= 0;
        lg[j] = ok ? sc[j >> 2][j & 3] * scale : -3.0e38f;
        mx = fmaxf(mx, lg[j]);
      }
      mx = fmaxf(mx, __shfl_xor(mx, 16));
      mx = fmaxf(mx, __shfl_xor(mx, 32));
      float sum = 0.f;
#pragma unroll
      for (int j = 0; j < 8; ++j) {
        lg[j] = lg[j] > -1.0e38f ? __expf(lg[j] - mx) : 0.f;
        sum += lg[j];
      }
      sum += __shfl_xor(sum, 16);
      sum += __shfl_xor(sum, 32);
      const float inv = __builtin_amdgcn_rcpf(sum);
      const bf16x8 pf = __builtin_bit_cast(bf16x8, u32x4{pack2(lg[0] * inv, lg[1] * inv), pack2(lg[2] * inv, lg[3] * inv),
                                                         pack2(lg[4] * inv, lg[5] * inv), pack2(lg[6] * inv, lg[7] * inv)});
      const int tok = tok0 + 16 * mq + c;
#pragma unroll
      for (int t = 0; t < 2; ++t) {
        const unsigned va = (16 * (mq - 1) + 4 * q + a) * QROW + 320 + 40 * hh + 32 * t + 8 * p;
        const bf16x8 vf = tr_frag(va, va + 16 * QROW);
        f32x4 o = __builtin_amdgcn_mfma_f32_16x16x32_bf16(vf, pf, f32x4{0.f, 0.f, 0.f, 0.f}, 0, 0, 0);
        const int col = 80 * hg + 20 * hh + 16 * t + 4 * q;
        const int G = col >> 4;
        if ((t == 0 || q == 0) && tok < S_)
          *(u32x2*)(a2 + (size_t)(((tok >> 6) * NKS2 + (G >> 1)) * 4 + ((tok >> 4) & 3)) * 1024 +
                    (16 * ((col >> 2) & 3) + c) * 16 + 8 * (G & 1)) = u32x2{pack2(o[0], o[1]), pack2(o[2], o[3])};
      }
    }
  }
}

__global__ __launch_bounds__(512, 2) void k1_kernel(const __hip_bfloat16* __restrict__ x,
                                                    const __hip_bfloat16* __restrict__ wqkv,
                                                    const __hip_bfloat16* __restrict__ wo,
                                                    const __hip_bfloat16* __restrict__ wg,
                                                    const __hip_bfloat16* __restrict__ wu,
                                                    const __hip_bfloat16* __restrict__ wd,
                                                    char* __restrict__ ws) {
  const int wave = __builtin_amdgcn_readfirstlane(threadIdx.x >> 6);
  if (wave < 4) k1_body<0>(x, wqkv, wo, wg, wu, wd, ws, wave);
  else k1_body<1>(x, wqkv, wo, wg, wu, wd, ws, wave);
}

constexpr int K2_DEPTH = 6;
__global__ __launch_bounds__(256, 1) void k2_kernel(const __hip_bfloat16* __restrict__ x,
                                                    const char* __restrict__ ws,
                                                    __hip_bfloat16* __restrict__ out) {
  const int tid = threadIdx.x, lane = tid & 63;
  const int wave = __builtin_amdgcn_readfirstlane(tid >> 6);
  const int q = lane >> 4, c = lane & 15;
  const int xcd = blockIdx.x & 7, idx = blockIdx.x >> 3;
  const int rb = (xcd >> 1) * 8 + (idx >> 2), cb = (xcd & 1) * 4 + (idx & 3);

  const char* ap = ws + WS_A2 + (size_t)(rb * NKS2 + 17 * wave) * 4096 + lane * 16;
  const char* wp = ws + WS_W2 + (size_t)(cb * NKS2 + 17 * wave) * 5120 + lane * 16;

  bf16x8 fa[K2_DEPTH][4], fw[K2_DEPTH][5];
  f32x4 acc[4][5];
#pragma unroll
  for (int m = 0; m < 4; ++m)
#pragma unroll
    for (int n = 0; n < 5; ++n) acc[m][n] = f32x4{0.f, 0.f, 0.f, 0.f};

  auto ld = [&](int i) {
    const int s = i % K2_DEPTH;
#pragma unroll
    for (int m = 0; m < 4; ++m) fa[s][m] = *(const bf16x8*)(ap + i * 4096 + m * 1024);
#pragma unroll
    for (int n = 0; n < 5; ++n) fw[s][n] = *(const bf16x8*)(wp + i * 5120 + n * 1024);
  };
#pragma unroll
  for (int i = 0; i < K2_DEPTH; ++i) ld(i);
  __builtin_amdgcn_sched_barrier(0);
#pragma unroll
  for (int i = 0; i < 17; ++i) {
    const int s = i % K2_DEPTH;
#pragma unroll
    for (int m = 0; m < 4; ++m)
#pragma unroll
      for (int n = 0; n < 5; ++n)
        acc[m][n] = __builtin_amdgcn_mfma_f32_16x16x32_bf16(fw[s][n], fa[s][m], acc[m][n], 0, 0, 0);
    __builtin_amdgcn_sched_barrier(0);
    if (i + K2_DEPTH < 17) ld(i + K2_DEPTH);
    __builtin_amdgcn_sched_barrier(0);
  }

  const int tok = rb * 64 + wave * 16 + c;
  const size_t obase = (size_t)tok * D_ + cb * 80;
  const u32x4 x0 = *(const u32x4*)(x + obase + 8 * q);
  const u32x4 x1 = *(const u32x4*)(x + obase + 32 + 8 * q);
  const u32x2 x2 = *(const u32x2*)(x + obase + 64 + 4 * q);

#pragma unroll
  for (int m = 0; m < 4; ++m) {
    if (m != wave) {
      const int si = wave < m ? wave : wave - 1;
#pragma unroll
      for (int n = 0; n < 5; ++n) *LDSP(f32x4, ((m * 3 + si) * 5 + n) * 1024 + lane * 16) = acc[m][n];
    }
  }
  __syncthreads();
  f32x4 r[5];
#pragma unroll
  for (int n = 0; n < 5; ++n) {
    r[n] = wave == 0 ? acc[0][n] : (wave == 1 ? acc[1][n] : (wave == 2 ? acc[2][n] : acc[3][n]));
#pragma unroll
    for (int si = 0; si < 3; ++si) r[n] += *LDSP(f32x4, ((wave * 3 + si) * 5 + n) * 1024 + lane * 16);
  }
  auto lo = [](unsigned v) { return __builtin_bit_cast(float, v << 16); };
  auto hi = [](unsigned v) { return __builtin_bit_cast(float, v & 0xFFFF0000u); };
  u32x4 o0 = {pack2(r[0][0] + lo(x0[0]), r[0][1] + hi(x0[0])), pack2(r[0][2] + lo(x0[1]), r[0][3] + hi(x0[1])),
              pack2(r[1][0] + lo(x0[2]), r[1][1] + hi(x0[2])), pack2(r[1][2] + lo(x0[3]), r[1][3] + hi(x0[3]))};
  u32x4 o1 = {pack2(r[2][0] + lo(x1[0]), r[2][1] + hi(x1[0])), pack2(r[2][2] + lo(x1[1]), r[2][3] + hi(x1[1])),
              pack2(r[3][0] + lo(x1[2]), r[3][1] + hi(x1[2])), pack2(r[3][2] + lo(x1[3]), r[3][3] + hi(x1[3]))};
  u32x2 o2 = {pack2(r[4][0] + lo(x2[0]), r[4][1] + hi(x2[0])), pack2(r[4][2] + lo(x2[1]), r[4][3] + hi(x2[1]))};
  *(u32x4*)(out + obase + 8 * q) = o0;
  *(u32x4*)(out + obase + 32 + 8 * q) = o1;
  *(u32x2*)(out + obase + 64 + 4 * q) = o2;
}

extern "C" void kernel_launch(void* const* d_in, const int* in_sizes, int n_in,
                              void* d_out, int out_size, void* d_ws, size_t ws_size,
                              hipStream_t stream) {
  const __hip_bfloat16* x = (const __hip_bfloat16*)d_in[0];
  const __hip_bfloat16* wqkv = (const __hip_bfloat16*)d_in[1];
  const __hip_bfloat16* wo = (const __hip_bfloat16*)d_in[2];
  const __hip_bfloat16* wg = (const __hip_bfloat16*)d_in[4];
  const __hip_bfloat16* wu = (const __hip_bfloat16*)d_in[5];
  const __hip_bfloat16* wd = (const __hip_bfloat16*)d_in[6];
  (void)hipFuncSetAttribute((const void*)k1_kernel, hipFuncAttributeMaxDynamicSharedMemorySize, LDS_K1);
  k1_kernel<<<256, 512, LDS_K1, stream>>>(x, wqkv, wo, wg, wu, wd, (char*)d_ws);
  k2_kernel<<<256, 256, 61440, stream>>>(x, (const char*)d_ws, (__hip_bfloat16*)d_out);
}
```

```cpp
#include <hip/hip_runtime.h>
#include <hip/hip_bf16.h>
#include <type_traits>

typedef short s16x4 __attribute__((ext_vector_type(4)));
typedef short s16x8 __attribute__((ext_vector_type(8)));
typedef __bf16 bf16x2 __attribute__((ext_vector_type(2)));
typedef __bf16 bf16x8 __attribute__((ext_vector_type(8)));
typedef float f32x2 __attribute__((ext_vector_type(2)));
typedef float f32x4 __attribute__((ext_vector_type(4)));
typedef unsigned u32x2 __attribute__((ext_vector_type(2)));
typedef unsigned u32x4 __attribute__((ext_vector_type(4)));
#define LDSP(T, a) ((__attribute__((address_space(3))) T*)(size_t)(unsigned)(a))

constexpr int S_ = 2048, D_ = 640, F_ = 1536;
constexpr int NKS2 = 68;
constexpr size_t WS_A2 = 0;
constexpr size_t WS_W2 = 9437184;

constexpr int A_SLOT = 22528;
constexpr int LDS_A = 0;
constexpr int B_SLOT = 16384;
constexpr int LDS_B = 3 * A_SLOT;
constexpr int LDS_ST = LDS_B + 5 * B_SLOT;
constexpr int LDS_CS = LDS_ST + 4 * 176 * 8;
constexpr int LDS_DUMMY = LDS_CS + 1024;
constexpr int LDS_K1 = LDS_DUMMY + 1024;
constexpr int QROW = 488;

__device__ __forceinline__ void dma16(const void* sbase, unsigned voff, unsigned lds_addr) {
  unsigned keep;
  asm volatile("s_mov_b32 %0, m0\n\ts_mov_b32 m0, %3\n\ts_nop 0\n\tglobal_load_lds_dwordx4 %1, %2\n\ts_mov_b32 m0, %0"
               : "=&s"(keep) : "v"(voff), "s"(sbase), "s"(lds_addr) : "memory");
}
template <int N> __device__ __forceinline__ void wait_vm() { asm volatile("s_waitcnt vmcnt(%0)" ::"i"(N) : "memory"); }
__device__ __forceinline__ void wait_lgkm0() { asm volatile("s_waitcnt lgkmcnt(0)" ::: "memory"); }

__device__ __forceinline__ bf16x8 tr_frag(unsigned a0, unsigned a1) {
  s16x4 lo = __builtin_amdgcn_ds_read_tr16_b64_v4i16(LDSP(s16x4, a0));
  s16x4 hi = __builtin_amdgcn_ds_read_tr16_b64_v4i16(LDSP(s16x4, a1));
  s16x8 v = __builtin_shufflevector(lo, hi, 0, 1, 2, 3, 4, 5, 6, 7);
  return __builtin_bit_cast(bf16x8, v);
}
__device__ __forceinline__ void mfma_acc(f32x4& c, const bf16x8& a, const bf16x8& b) {
  asm volatile("v_mfma_f32_16x16x32_bf16 %0, %1, %2, %0" : "+a"(c) : "v"(a), "v"(b));
}
__device__ __forceinline__ float red_max4(float a) {
  unsigned u = __builtin_bit_cast(unsigned, a);
  auto r = __builtin_amdgcn_permlane16_swap(u, u, false, false);
  a = fmaxf(__builtin_bit_cast(float, r[0]), __builtin_bit_cast(float, r[1]));
  u = __builtin_bit_cast(unsigned, a);
  auto r2 = __builtin_amdgcn_permlane32_swap(u, u, false, false);
  return fmaxf(__builtin_bit_cast(float, r2[0]), __builtin_bit_cast(float, r2[1]));
}
__device__ __forceinline__ float red_sum4(float a) {
  unsigned u = __builtin_bit_cast(unsigned, a);
  auto r = __builtin_amdgcn_permlane16_swap(u, u, false, false);
  a = __builtin_bit_cast(float, r[0]) + __builtin_bit_cast(float, r[1]);
  u = __builtin_bit_cast(unsigned, a);
  auto r2 = __builtin_amdgcn_permlane32_swap(u, u, false, false);
  return __builtin_bit_cast(float, r2[0]) + __builtin_bit_cast(float, r2[1]);
}
__device__ __forceinline__ unsigned pack2(float a, float b) {
  bf16x2 p = {(__bf16)a, (__bf16)b};
  return __builtin_bit_cast(unsigned, p);
}

#define STAMP(i)
#define STAMP_DECL
#define STAMP_OUT(k)
template <int I, int N, class Fn> __device__ __forceinline__ void static_for(Fn&& f) {
  if constexpr (I < N) { f(std::integral_constant<int, I>{}); static_for<I + 1, N>(f); }
}

#define SB() __builtin_amdgcn_sched_barrier(0)

__global__ __launch_bounds__(512, 2) void k1_kernel(const __hip_bfloat16* __restrict__ x,
                                                    const __hip_bfloat16* __restrict__ wqkv,
                                                    const __hip_bfloat16* __restrict__ wo,
                                                    const __hip_bfloat16* __restrict__ wg,
                                                    const __hip_bfloat16* __restrict__ wu,
                                                    const __hip_bfloat16* __restrict__ wd,
                                                    char* __restrict__ ws) {
  STAMP_DECL;
  STAMP(0);
  const int tid = threadIdx.x, lane = tid & 63;
  const int wave = __builtin_amdgcn_readfirstlane(tid >> 6);
  const int mh = wave >> 2, wq = wave & 3;
  const int q = lane >> 4, c = lane & 15;

  const int xcd = blockIdx.x & 7, idx = blockIdx.x >> 3;
  const int xr = xcd >> 2, xc = xcd & 3;
  const int natt = xr == 0 ? 14 : 12;
  const bool is_att = idx < natt;
  int tok0, cbk = 0, hg = 0, job = 1000;
  if (is_att) {
    const int ab = (xr == 0 ? 0 : 7) + (idx >> 1);
    hg = 2 * xc + (idx & 1);
    tok0 = 160 * ab - 16;
  } else {
    const int j = idx - natt;
    if (j >= 18) return;
    const int rb = 6 * xr + j / 3;
    cbk = 3 * xc + j % 3;
    tok0 = 175 * rb - 1;
    job = rb * 12 + cbk;
  }

  unsigned b_voff[2];
  const char* b_base[2];
  int b_step;
  {
    const int row = 4 * wave + (lane >> 4);
    const int hsw = (row & 3) | (((row >> 3) & 1) << 2);
    const int g = (lane & 15) ^ (hsw << 1);
    if (is_att) {
#pragma unroll
      for (int e = 0; e < 2; ++e) {
        int L = 16 * e + g;
        L = L > 29 ? 29 : L;
        const int sec = L / 10;
        b_voff[e] = (unsigned)(row * 3840 + (640 * sec + 80 * hg + 8 * (L - 10 * sec)) * 2);
      }
      b_base[0] = (const char*)wqkv; b_base[1] = (const char*)wqkv;
      b_step = 32 * 3840;
    } else {
      b_voff[0] = b_voff[1] = (unsigned)(row * 3072 + (128 * cbk + 8 * g) * 2);
      b_base[0] = (const char*)wg; b_base[1] = (const char*)wu;
      b_step = 32 * 3072;
    }
  }
  auto issueB1 = [&](int e, int j, unsigned slot_off) {
    const bool ok = j <= 19;
    dma16(b_base[e] + (size_t)(ok ? j : 0) * b_step, b_voff[e], ok ? LDS_B + slot_off + e * 8192 + wave * 1024 : LDS_DUMMY);
  };
  issueB1(0, 0, 0); issueB1(1, 0, 0);

  unsigned a_voff[3];
#pragma unroll
  for (int e = 0; e < 3; ++e) {
    const int ii = (wave + 8 * e) < 22 ? wave + 8 * e : wave + 8;
    const int row = 8 * ii + (lane >> 3);
    int tok = tok0 + row;
    tok = tok < 0 ? 0 : (tok > S_ - 1 ? S_ - 1 : tok);
    a_voff[e] = (unsigned)(tok * 1280 + (((lane & 7) ^ ((row >> 1) & 7)) << 4));
  }
  unsigned a_dst2 = wave < 6 ? 16384u : 8192u;
  const char* a_base = (const char*)x;
  bool a_on0 = wave != 0 || lane >= 8;
  auto issueA1 = [&](int e, const char* ab, unsigned slot_off, bool ok = true) {
    const unsigned dst = LDS_A + slot_off + wave * 1024;
    if (e == 0) { if (a_on0) dma16(ab, a_voff[0], ok ? dst : LDS_DUMMY); }
    else if (e == 1) dma16(ab, a_voff[1], ok ? dst + 8192 : LDS_DUMMY);
    else dma16(ab, a_voff[2], ok ? dst + a_dst2 : LDS_DUMMY);
  };
#pragma unroll
  for (int e = 0; e < 3; ++e) issueA1(e, a_base, 0);
  issueB1(0, 1, B_SLOT); issueB1(1, 1, B_SLOT);
  issueB1(0, 2, 2 * B_SLOT); issueB1(1, 2, 2 * B_SLOT);
#pragma unroll
  for (int e = 0; e < 3; ++e) issueA1(e, a_base + 128, A_SLOT);
  issueB1(0, 3, 3 * B_SLOT); issueB1(1, 3, 3 * B_SLOT);
  issueB1(0, 4, 4 * B_SLOT); issueB1(1, 4, 4 * B_SLOT);
#pragma unroll
  for (int e = 0; e < 3; ++e) issueA1(e, a_base + 256, 2 * A_SLOT);
  a_base += 384;
  if (tid < 24) {
    u32x4 ones = {0x3F803F80u, 0x3F803F80u, 0x3F803F80u, 0x3F803F80u};
    *LDSP(u32x4, LDS_A + (tid >> 3) * A_SLOT + (tid & 7) * 16) = ones;
  }

  unsigned a_rd[2];
#pragma unroll
  for (int s = 0; s < 2; ++s)
    a_rd[s] = LDS_A + mh * 6 * 2048 + c * 128 + (((4 * s + q) ^ ((c >> 1) & 7)) << 4);
  unsigned b_rd[4];
  int ntile[4];
  {
    const int a = c >> 2, p = c & 3;
    const int hh = a | ((q & 1) << 2);
#pragma unroll
    for (int n = 0; n < 4; ++n) {
      const int nt = is_att ? 4 * wq + n : (n < 2 ? 2 * wq + n : 8 + 2 * wq + n - 2);
      ntile[n] = nt;
      b_rd[n] = LDS_B + (nt >> 3) * 8192 + (8 * q + a) * 256 + (((nt & 7) ^ hh) << 5) + 8 * p;
    }
  }

  f32x4 acc[6][4];
#pragma unroll
  for (int m = 0; m < 6; ++m)
#pragma unroll
    for (int n = 0; n < 4; ++n) acc[m][n] = f32x4{0.f, 0.f, 0.f, 0.f};
  float ssum[3] = {0.f, 0.f, 0.f}, ssq[3] = {0.f, 0.f, 0.f};
  u32x4 sd[3];
  const unsigned s_rd = LDS_A + wave * 22 * 128 + lane * 16;

  bf16x8 af[2][6], bfr[2][4];
  auto ld_b = [&](auto par, int n, unsigned bo) {
    bfr[decltype(par)::value][n] = tr_frag(b_rd[n] + bo, b_rd[n] + bo + 1024);
  };
  auto ld_a = [&](auto par, int m, unsigned ao) {
    af[decltype(par)::value][m] = *LDSP(bf16x8, a_rd[decltype(par)::value] + ao + m * 2048);
  };
  auto st_rd = [&](int i, unsigned ao) { sd[i] = *LDSP(u32x4, s_rd + ao + i * 1024); };
  auto st_acc = [&](int i, int j) {
    const unsigned v = sd[i][j];
    const float lo = __builtin_bit_cast(float, v << 16), hi = __builtin_bit_cast(float, v & 0xFFFF0000u);
    ssum[i] += lo; ssq[i] = __builtin_fmaf(lo, lo, ssq[i]);
    ssum[i] += hi; ssq[i] = __builtin_fmaf(hi, hi, ssq[i]);
  };
  auto step = [&](auto par, unsigned ao, unsigned bo, auto dma) {
    constexpr int P = decltype(par)::value;
    constexpr int NDMA = P == 0 ? 2 : 5;
    using NX = std::integral_constant<int, 1 - P>;
    constexpr int NS = P == 0 ? 1 : 2;
    constexpr int NV = P == 0 ? 8 : 4;
    constexpr int NL = 10;
    constexpr int NF = NL + NS + NDMA + NV;
    constexpr int NMF = 20;
    auto filler = [&](auto fc) {
      constexpr int f = decltype(fc)::value;
      if constexpr (f < NL) {
        if constexpr (f < 4) ld_b(NX{}, f, bo); else ld_a(NX{}, f - 4, ao);
      } else if constexpr (f < NL + NS) {
        if constexpr (P == 0) st_rd(2, ao); else st_rd(f - NL, ao);
      } else if constexpr (f < NL + NS + NDMA) {
        dma(f - NL - NS);
      } else {
        constexpr int v = f - NL - NS - NDMA;
        if constexpr (P == 0) st_acc(v >> 2, v & 3); else st_acc(2, v);
      }
    };
    wait_lgkm0();
    __builtin_amdgcn_s_barrier();
    SB();
    static_for<0, NMF>([&](auto ic) {
      constexpr int i = decltype(ic)::value;
      mfma_acc(acc[i >> 2][i & 3], bfr[P][i & 3], af[P][i >> 2]);
      SB();
      static_for<(i * NF) / NMF, ((i + 1) * NF) / NMF>(filler);
      SB();
    });
    if (mh == 0) {
#pragma unroll
      for (int n = 0; n < 4; ++n) mfma_acc(acc[5][n], bfr[P][n], af[P][5]);
    }
    SB();
  };
  using P0 = std::integral_constant<int, 0>;
  using P1 = std::integral_constant<int, 1>;

#pragma unroll
  for (int m = 0; m < 6; ++m)
#pragma unroll
    for (int n = 0; n < 4; ++n) asm volatile("" : "+a"(acc[m][n]));
  wait_vm<14>();
  wait_lgkm0();
  __builtin_amdgcn_s_barrier();
#pragma unroll
  for (int n = 0; n < 4; ++n) ld_b(P0{}, n, 0);
#pragma unroll
  for (int m = 0; m < 6; ++m) ld_a(P0{}, m, 0);
  st_rd(0, 0); st_rd(1, 0);
  STAMP(1);

  unsigned bo = 0;
  unsigned ao = 0;
#pragma unroll 1
  for (int t = 0; t < 20; t += 2) {
    const unsigned bo1 = bo == 4 * B_SLOT ? 0 : bo + B_SLOT;
    const unsigned bo2 = bo1 == 4 * B_SLOT ? 0 : bo1 + B_SLOT;
    const unsigned ao1 = ao == 2 * A_SLOT ? 0 : ao + A_SLOT;
    if (t == 14) {
      const int jb = job < 136 ? job : 0;
      const int ks = jb >> 1, half = jb & 1;
      a_base = ks < 20 ? (const char*)wo + (size_t)ks * 32 * 1280 : (const char*)wd + (size_t)(ks - 20) * 32 * 1280;
      a_dst2 = wave < 4 ? 16384u : 8192u;
      a_on0 = true;
#pragma unroll
      for (int e = 0; e < 3; ++e) {
        const int pe = (wave + 8 * e) < 20 ? e : 1;
        const int o = 1024 * (wave + 8 * pe) + 16 * lane;
        const int row = o / 640;
        a_voff[e] = (unsigned)(row * 1280 + 640 * half + (o - row * 640));
      }
    }
    wait_vm<12>();
    step(P0{}, ao, bo1, [&](int e) { issueB1(e, t + 5, bo); });
    wait_vm<9>();
    step(P1{}, ao1, bo2, [&](int e) {
      if (e < 2) issueB1(e, t + 6, bo1);
      else issueA1(e - 2, a_base, ao, t <= 14);
    });
    a_base += t < 14 ? 128 : 0;
    bo = bo2; ao = ao1;
  }
  asm volatile("s_nop 15\n\ts_nop 15" ::: "memory");
  STAMP(6);

#pragma unroll
  for (int i = 0; i < 3; ++i) {
    float a = ssum[i], b = ssq[i];
    a += __shfl_xor(a, 1); b += __shfl_xor(b, 1);
    a += __shfl_xor(a, 2); b += __shfl_xor(b, 2);
    a += __shfl_xor(a, 4); b += __shfl_xor(b, 4);
    if ((lane & 7) == 0 && 64 * i + lane < 176) {
      const float mu = a * (1.0f / D_);
      float var = b * (1.0f / D_) - mu * mu;
      var = var < 0.f ? 0.f : var;
      const float rs = __builtin_amdgcn_rsqf(var + 1e-5f);
      *LDSP(f32x2, LDS_ST + (wave * 22 + 8 * i + (lane >> 3)) * 8) = f32x2{rs, -mu * rs};
    }
  }
  if (mh == 0 && c == 0) {
#pragma unroll
    for (int n = 0; n < 4; ++n) *LDSP(f32x4, LDS_CS + (ntile[n] * 16 + 4 * q) * 4) = acc[0][n];
  }
  wait_vm<0>();
  wait_lgkm0();
  __builtin_amdgcn_s_barrier();

  f32x4 cs[4];
#pragma unroll
  for (int n = 0; n < 4; ++n) cs[n] = *LDSP(f32x4, LDS_CS + (ntile[n] * 16 + 4 * q) * 4);
  float rstd[6], nmr[6];
#pragma unroll
  for (int m = 0; m < 6; ++m) {
    const f32x2 v = *LDSP(f32x2, LDS_ST + ((6 * mh + m) * 16 + c) * 8);
    rstd[m] = v[0]; nmr[m] = v[1];
  }

  char* a2 = ws + WS_A2;
  STAMP(7);
  if (!is_att) {
    const int ks = 20 + 4 * cbk + wq;
    constexpr float NL2E = -1.4426950408889634f;
#pragma unroll
    for (int m = 0; m < 6; ++m) {
      if (m < 5 || mh == 0) {
        const int trow = (6 * mh + m) * 16 + c;
        const int tok = tok0 + trow;
        const float rg = rstd[m] * NL2E, ng = nmr[m] * NL2E;
        float hv[8];
#pragma unroll
        for (int n = 0; n < 2; ++n)
#pragma unroll
          for (int r = 0; r < 4; ++r) {
            const float e = __builtin_amdgcn_exp2f(__builtin_fmaf(acc[m][n][r], rg, ng * cs[n][r]));
            const float u = __builtin_fmaf(acc[m][n + 2][r], rstd[m], nmr[m] * cs[n + 2][r]);
            hv[4 * n + r] = u * __builtin_amdgcn_rcpf(1.0f + e);
          }
        u32x4 pk = {pack2(hv[0], hv[1]), pack2(hv[2], hv[3]), pack2(hv[4], hv[5]), pack2(hv[6], hv[7])};
        if (trow >= 1 && tok < S_)
          *(u32x4*)(a2 + (size_t)(((tok >> 6) * NKS2 + ks) * 4 + ((tok >> 4) & 3)) * 1024 + (16 * q + (tok & 15)) * 16) = pk;
      }
    }
    if (job < 136) {
      const int ks2 = job >> 1, half = job & 1;
      const int a = c >> 2, p = c & 3;
#pragma unroll 1
      for (int u = wave; u < 20; u += 8) {
        const int cl = u / 5, n5 = u - 5 * cl;
        const int colst = n5 < 4 ? 32 * (n5 >> 1) + 8 * p + 4 * (n5 & 1) : 64 + 4 * p;
        const unsigned ad = LDS_A + 1 * A_SLOT + (4 * q + a) * 640 + (80 * cl + colst) * 2;
        bf16x8 f = tr_frag(ad, ad + 16 * 640);
        const int cb2 = 4 * half + cl;
        *(bf16x8*)(ws + WS_W2 + (size_t)((cb2 * NKS2 + ks2) * 5 + n5) * 1024 + lane * 16) = f;
      }
    }
  } else {
#pragma unroll
    for (int m = 0; m < 6; ++m) {
      if (m < 5 || mh == 0) {
        const int trow = (6 * mh + m) * 16 + c;
#pragma unroll
        for (int n = 0; n < 4; ++n) {
          float y[4];
#pragma unroll
          for (int r = 0; r < 4; ++r) y[r] = __builtin_fmaf(acc[m][n][r], rstd[m], nmr[m] * cs[n][r]);
          if (n < 3 || wq < 3)
            *LDSP(u32x2, trow * QROW + (ntile[n] * 16 + 4 * q) * 2) = u32x2{pack2(y[0], y[1]), pack2(y[2], y[3])};
        }
      }
    }
    float bias[8];
#pragma unroll
    for (int j = 0; j < 8; ++j) {
      const int diff = 16 + c - (16 * (j >> 2) + 4 * q + (j & 3));
      bias[j] = (diff >= 0 && diff <= 14 && (diff & 1) == 0) ? 0.f : -3.0e38f;
    }
    const float sl2 = 0.22360679774997896f * 1.4426950408889634f;
    const int dofs = q < 2 ? 16 * q : 32;
    const unsigned qk_l = c * QROW + dofs;
    const unsigned v_l = (4 * q + (c >> 2)) * QROW + 320 + 8 * (c & 3);
    wait_lgkm0();
    __builtin_amdgcn_s_barrier();
    STAMP(8);
#pragma unroll 1
    for (int unit = wave * 5; unit < wave * 5 + 5; ++unit) {
      const int mq = 1 + (unit >> 2), hh = unit & 3;
      const unsigned ub = 16 * (mq - 1) * QROW + 40 * hh;
      u32x2 q0 = *LDSP(u32x2, ub + qk_l + 16 * QROW), q1 = *LDSP(u32x2, ub + qk_l + 16 * QROW + 8);
      u32x2 k00 = *LDSP(u32x2, ub + qk_l + 160), k01 = *LDSP(u32x2, ub + qk_l + 168);
      u32x2 k10 = *LDSP(u32x2, ub + qk_l + 16 * QROW + 160), k11 = *LDSP(u32x2, ub + qk_l + 16 * QROW + 168);
      const bf16x8 vf0 = tr_frag(ub + v_l, ub + v_l + 16 * QROW);
      const bf16x8 vf1 = tr_frag(ub + v_l + 32, ub + v_l + 32 + 16 * QROW);
      if (q >= 2) { q1 = u32x2{0u, 0u}; }
      if (q == 3) { q0 = u32x2{0u, 0u}; }
      const bf16x8 qf = __builtin_bit_cast(bf16x8, u32x4{q0[0], q0[1], q1[0], q1[1]});
      const f32x4 z4 = {0.f, 0.f, 0.f, 0.f};
      const f32x4 s0 = __builtin_amdgcn_mfma_f32_16x16x32_bf16(__builtin_bit_cast(bf16x8, u32x4{k00[0], k00[1], k01[0], k01[1]}), qf, z4, 0, 0, 0);
      const f32x4 s1 = __builtin_amdgcn_mfma_f32_16x16x32_bf16(__builtin_bit_cast(bf16x8, u32x4{k10[0], k10[1], k11[0], k11[1]}), qf, z4, 0, 0, 0);
      float lg[8];
#pragma unroll
      for (int j = 0; j < 4; ++j) {
        lg[j] = __builtin_fmaf(s0[j], sl2, bias[j]);
        lg[4 + j] = __builtin_fmaf(s1[j], sl2, bias[4 + j]);
      }
      if (tok0 + 16 * (mq - 1) < 0) {
#pragma unroll
        for (int j = 0; j < 4; ++j) lg[j] = -3.0e38f;
      }
      float mx = fmaxf(fmaxf(fmaxf(lg[0], lg[1]), fmaxf(lg[2], lg[3])), fmaxf(fmaxf(lg[4], lg[5]), fmaxf(lg[6], lg[7])));
      mx = fmaxf(mx, __shfl_xor(mx, 16)); mx = fmaxf(mx, __shfl_xor(mx, 32));
      float sum = 0.f;
#pragma unroll
      for (int j = 0; j < 8; ++j) { lg[j] = __builtin_amdgcn_exp2f(lg[j] - mx); sum += lg[j]; }
      const float inv = __builtin_amdgcn_rcpf((sum += __shfl_xor(sum, 16), sum += __shfl_xor(sum, 32), sum));
      const bf16x8 pf = __builtin_bit_cast(bf16x8, u32x4{pack2(lg[0] * inv, lg[1] * inv), pack2(lg[2] * inv, lg[3] * inv),
                                                         pack2(lg[4] * inv, lg[5] * inv), pack2(lg[6] * inv, lg[7] * inv)});
      const f32x4 o0 = __builtin_amdgcn_mfma_f32_16x16x32_bf16(vf0, pf, z4, 0, 0, 0);
      const f32x4 o1 = __builtin_amdgcn_mfma_f32_16x16x32_bf16(vf1, pf, z4, 0, 0, 0);
      const int tokb = tok0 + 16 * mq;
      if (tokb < S_) {
        char* ob = a2 + (size_t)((tokb >> 6) * NKS2 * 4 + ((tokb >> 4) & 3)) * 1024 + c * 16;
        const int col0 = 80 * hg + 20 * hh + 4 * q;
        *(u32x2*)(ob + (col0 >> 5) * 4096 + ((col0 >> 2) & 3) * 256 + ((col0 >> 4) & 1) * 8) = u32x2{pack2(o0[0], o0[1]), pack2(o0[2], o0[3])};
        const int col1 = 80 * hg + 20 * hh + 16;
        if (q == 0)
          *(u32x2*)(ob + (col1 >> 5) * 4096 + ((col1 >> 2) & 3) * 256 + ((col1 >> 4) & 1) * 8) = u32x2{pack2(o1[0], o1[1]), pack2(o1[2], o1[3])};
      }
    }
  }
  STAMP(9);
  STAMP(10);
  STAMP_OUT(0);
}

constexpr int K2_DEPTH = 6;
__global__ __launch_bounds__(256, 1) void k2_kernel(const __hip_bfloat16* __restrict__ x,
                                                    char* __restrict__ ws,
                                                    __hip_bfloat16* __restrict__ out) {
  const int tid = threadIdx.x, lane = tid & 63;
  const int wave = __builtin_amdgcn_readfirstlane(tid >> 6);
  const int q = lane >> 4, c = lane & 15;
  const int xcd = blockIdx.x & 7, idx = blockIdx.x >> 3;
  const int rb = (xcd >> 1) * 8 + (idx >> 2), cb = (xcd & 1) * 4 + (idx & 3);
  STAMP_DECL;
  STAMP(0);

  const char* ap = ws + WS_A2 + (size_t)(rb * NKS2 + 17 * wave) * 4096 + lane * 16;
  const char* wp = ws + WS_W2 + (size_t)(cb * NKS2 + 17 * wave) * 5120 + lane * 16;

  bf16x8 fa[K2_DEPTH][4], fw[K2_DEPTH][5];
  f32x4 acc[4][5];
#pragma unroll
  for (int m = 0; m < 4; ++m)
#pragma unroll
    for (int n = 0; n < 5; ++n) acc[m][n] = f32x4{0.f, 0.f, 0.f, 0.f};

  auto ld = [&](int i) {
    const int s = i % K2_DEPTH;
#pragma unroll
    for (int m = 0; m < 4; ++m) fa[s][m] = *(const bf16x8*)(ap + i * 4096 + m * 1024);
#pragma unroll
    for (int n = 0; n < 5; ++n) fw[s][n] = *(const bf16x8*)(wp + i * 5120 + n * 1024);
  };
#pragma unroll
  for (int i = 0; i < K2_DEPTH; ++i) ld(i);
  __builtin_amdgcn_sched_barrier(0);
#pragma unroll
  for (int i = 0; i < 17; ++i) {
    const int s = i % K2_DEPTH;
#pragma unroll
    for (int m = 0; m < 4; ++m)
#pragma unroll
      for (int n = 0; n < 5; ++n)
        acc[m][n] = __builtin_amdgcn_mfma_f32_16x16x32_bf16(fw[s][n], fa[s][m], acc[m][n], 0, 0, 0);
    __builtin_amdgcn_sched_barrier(0);
    if (i + K2_DEPTH < 17) ld(i + K2_DEPTH);
    __builtin_amdgcn_sched_barrier(0);
  }

  const int tok = rb * 64 + wave * 16 + c;
  const size_t obase = (size_t)tok * D_ + cb * 80;
  const u32x4 x0 = *(const u32x4*)(x + obase + 8 * q);
  const u32x4 x1 = *(const u32x4*)(x + obase + 32 + 8 * q);
  const u32x2 x2 = *(const u32x2*)(x + obase + 64 + 4 * q);

#pragma unroll
  for (int m = 0; m < 4; ++m) {
    if (m != wave) {
      const int si = wave < m ? wave : wave - 1;
#pragma unroll
      for (int n = 0; n < 5; ++n) *LDSP(f32x4, ((m * 3 + si) * 5 + n) * 1024 + lane * 16) = acc[m][n];
    }
  }
  __syncthreads();
  f32x4 r[5];
#pragma unroll
  for (int n = 0; n < 5; ++n) {
    r[n] = wave == 0 ? acc[0][n] : (wave == 1 ? acc[1][n] : (wave == 2 ? acc[2][n] : acc[3][n]));
#pragma unroll
    for (int si = 0; si < 3; ++si) r[n] += *LDSP(f32x4, ((wave * 3 + si) * 5 + n) * 1024 + lane * 16);
  }
  auto lo = [](unsigned v) { return __builtin_bit_cast(float, v << 16); };
  auto hi = [](unsigned v) { return __builtin_bit_cast(float, v & 0xFFFF0000u); };
  u32x4 o0 = {pack2(r[0][0] + lo(x0[0]), r[0][1] + hi(x0[0])), pack2(r[0][2] + lo(x0[1]), r[0][3] + hi(x0[1])),
              pack2(r[1][0] + lo(x0[2]), r[1][1] + hi(x0[2])), pack2(r[1][2] + lo(x0[3]), r[1][3] + hi(x0[3]))};
  u32x4 o1 = {pack2(r[2][0] + lo(x1[0]), r[2][1] + hi(x1[0])), pack2(r[2][2] + lo(x1[1]), r[2][3] + hi(x1[1])),
              pack2(r[3][0] + lo(x1[2]), r[3][1] + hi(x1[2])), pack2(r[3][2] + lo(x1[3]), r[3][3] + hi(x1[3]))};
  u32x2 o2 = {pack2(r[4][0] + lo(x2[0]), r[4][1] + hi(x2[0])), pack2(r[4][2] + lo(x2[1]), r[4][3] + hi(x2[1]))};
  *(u32x4*)(out + obase + 8 * q) = o0;
  *(u32x4*)(out + obase + 32 + 8 * q) = o1;
  *(u32x2*)(out + obase + 64 + 4 * q) = o2;
  STAMP(6);
  STAMP_OUT(1);
}

extern "C" void kernel_launch(void* const* d_in, const int* in_sizes, int n_in,
                              void* d_out, int out_size, void* d_ws, size_t ws_size,
                              hipStream_t stream) {
  const __hip_bfloat16* x = (const __hip_bfloat16*)d_in[0];
  const __hip_bfloat16* wqkv = (const __hip_bfloat16*)d_in[1];
  const __hip_bfloat16* wo = (const __hip_bfloat16*)d_in[2];
  const __hip_bfloat16* wg = (const __hip_bfloat16*)d_in[4];
  const __hip_bfloat16* wu = (const __hip_bfloat16*)d_in[5];
  const __hip_bfloat16* wd = (const __hip_bfloat16*)d_in[6];
  (void)hipFuncSetAttribute((const void*)k1_kernel, hipFuncAttributeMaxDynamicSharedMemorySize, LDS_K1);
  k1_kernel<<<256, 512, LDS_K1, stream>>>(x, wqkv, wo, wg, wu, wd, (char*)d_ws);
  k2_kernel<<<256, 256, 61440, stream>>>(x, (char*)d_ws, (__hip_bfloat16*)d_out);
}
```

```cpp
#include <hip/hip_runtime.h>
#include <hip/hip_bf16.h>
#include <type_traits>

typedef short s16x4 __attribute__((ext_vector_type(4)));
typedef short s16x8 __attribute__((ext_vector_type(8)));
typedef __bf16 bf16x2 __attribute__((ext_vector_type(2)));
typedef __bf16 bf16x8 __attribute__((ext_vector_type(8)));
typedef float f32x2 __attribute__((ext_vector_type(2)));
typedef float f32x4 __attribute__((ext_vector_type(4)));
typedef unsigned u32x2 __attribute__((ext_vector_type(2)));
typedef unsigned u32x4 __attribute__((ext_vector_type(4)));
#define LDSP(T, a) ((__attribute__((address_space(3))) T*)(size_t)(unsigned)(a))

constexpr int S_ = 2048, D_ = 640, F_ = 1536;
constexpr int NKS2 = 68;
constexpr size_t WS_A2 = 0;
constexpr size_t WS_W2 = 9437184;

constexpr int A_SLOT = 22528;
constexpr int LDS_A = 0;
constexpr int B_SLOT = 16384;
constexpr int LDS_B = 3 * A_SLOT;
constexpr int LDS_ST = LDS_B + 5 * B_SLOT;
constexpr int LDS_CS = LDS_ST + 4 * 176 * 8;
constexpr int LDS_DUMMY = LDS_CS + 1024;
constexpr int LDS_K1 = LDS_DUMMY + 1024;
constexpr int QROW = 488;

__device__ __forceinline__ void dma16(const void* sbase, unsigned voff, unsigned lds_addr) {
  unsigned keep;
  asm volatile("s_mov_b32 %0, m0\n\ts_mov_b32 m0, %3\n\ts_nop 0\n\tglobal_load_lds_dwordx4 %1, %2\n\ts_mov_b32 m0, %0"
               : "=&s"(keep) : "v"(voff), "s"(sbase), "s"(lds_addr) : "memory");
}
template <int N> __device__ __forceinline__ void wait_vm() { asm volatile("s_waitcnt vmcnt(%0)" ::"i"(N) : "memory"); }
__device__ __forceinline__ void wait_lgkm0() {
  __builtin_amdgcn_s_waitcnt(0xC07F);
  asm volatile("" ::: "memory");
}

__device__ __forceinline__ bf16x8 tr_frag(unsigned a0, unsigned a1) {
  s16x4 lo = __builtin_amdgcn_ds_read_tr16_b64_v4i16(LDSP(s16x4, a0));
  s16x4 hi = __builtin_amdgcn_ds_read_tr16_b64_v4i16(LDSP(s16x4, a1));
  s16x8 v = __builtin_shufflevector(lo, hi, 0, 1, 2, 3, 4, 5, 6, 7);
  return __builtin_bit_cast(bf16x8, v);
}
__device__ __forceinline__ void mfma_acc(f32x4& c, const bf16x8& a, const bf16x8& b) {
  asm volatile("v_mfma_f32_16x16x32_bf16 %0, %1, %2, %0" : "+a"(c) : "v"(a), "v"(b));
}
__device__ __forceinline__ void mfma_accv(f32x4& c, const bf16x8& a, const bf16x8& b) {
  asm volatile("v_mfma_f32_16x16x32_bf16 %0, %1, %2, %0" : "+v"(c) : "v"(a), "v"(b));
}
__device__ __forceinline__ void swap16(float& a, float& b) {
  asm volatile("s_nop 1\n\tv_permlane16_swap_b32 %0, %1" : "+v"(a), "+v"(b));
}
__device__ __forceinline__ void swap32(float& a, float& b) {
  asm volatile("s_nop 1\n\tv_permlane32_swap_b32 %0, %1" : "+v"(a), "+v"(b));
}
__device__ __forceinline__ float red_max4(float a) {
  float b = a;
  swap16(a, b); a = fmaxf(a, b); b = a;
  swap32(a, b); return fmaxf(a, b);
}
__device__ __forceinline__ float red_sum4(float a) {
  float b = a;
  swap16(a, b); a = a + b; b = a;
  swap32(a, b); return a + b;
}
__device__ __forceinline__ unsigned pack2(float a, float b) {
  bf16x2 p = {(__bf16)a, (__bf16)b};
  return __builtin_bit_cast(unsigned, p);
}

#define STAMP(i)
#define STAMP_DECL
#define STAMP_OUT(k)
template <int I, int N, class Fn> __device__ __forceinline__ void static_for(Fn&& f) {
  if constexpr (I < N) { f(std::integral_constant<int, I>{}); static_for<I + 1, N>(f); }
}

#define SB() __builtin_amdgcn_sched_barrier(0)

__global__ __launch_bounds__(768, 3) void k1_kernel(const __hip_bfloat16* __restrict__ x,
                                                    const __hip_bfloat16* __restrict__ wqkv,
                                                    const __hip_bfloat16* __restrict__ wo,
                                                    const __hip_bfloat16* __restrict__ wg,
                                                    const __hip_bfloat16* __restrict__ wu,
                                                    const __hip_bfloat16* __restrict__ wd,
                                                    char* __restrict__ ws) {
  STAMP_DECL;
  STAMP(0);
  const int tid = threadIdx.x, lane = tid & 63;
  const int wave = __builtin_amdgcn_readfirstlane(tid >> 6);
  const int mh = (wave >> 2) & 1, wq = wave & 3;
  const int q = lane >> 4, c = lane & 15;

  const int xcd = blockIdx.x & 7, idx = blockIdx.x >> 3;
  const int xr = xcd >> 2, xc = xcd & 3;
  const int natt = xr == 0 ? 14 : 12;
  const bool is_att = idx < natt;
  int tok0, cbk = 0, hg = 0, job = 1000;
  if (is_att) {
    const int ab = (xr == 0 ? 0 : 7) + (idx >> 1);
    hg = 2 * xc + (idx & 1);
    tok0 = 160 * ab - 16;
  } else {
    const int j = idx - natt;
    if (j >= 18) return;
    const int rb = 6 * xr + j / 3;
    cbk = 3 * xc + j % 3;
    tok0 = 175 * rb - 1;
    job = rb * 12 + cbk;
  }
  int ntile[4];
#pragma unroll
  for (int n = 0; n < 4; ++n) ntile[n] = is_att ? 4 * wq + n : (n < 2 ? 2 * wq + n : 8 + 2 * wq + n - 2);
  f32x4 acc[6][4];

  if (wave >= 8) {
    const int L = wq;
    unsigned b_voff[4];
    const char* b_base[2];
    int b_step;
#pragma unroll
    for (int j = 0; j < 4; ++j) {
      const int row = 4 * (L + 4 * (j & 1)) + (lane >> 4);
      const int hsw = (row & 3) | (((row >> 3) & 1) << 2);
      const int g = (lane & 15) ^ (hsw << 1);
      if (is_att) {
        int Lc = 16 * (j >> 1) + g;
        Lc = Lc > 29 ? 29 : Lc;
        const int sec = Lc / 10;
        b_voff[j] = (unsigned)(row * 3840 + (640 * sec + 80 * hg + 8 * (Lc - 10 * sec)) * 2);
      } else {
        b_voff[j] = (unsigned)(row * 3072 + (128 * cbk + 8 * g) * 2);
      }
    }
    if (is_att) { b_base[0] = (const char*)wqkv; b_base[1] = (const char*)wqkv; b_step = 32 * 3840; }
    else { b_base[0] = (const char*)wg; b_base[1] = (const char*)wu; b_step = 32 * 3072; }
    auto issueB = [&](int j, unsigned slot_off) {
#pragma unroll
      for (int e = 0; e < 4; ++e)
        dma16(b_base[e >> 1] + (size_t)j * b_step, b_voff[e], LDS_B + slot_off + (L + 4 * e) * 1024);
    };
    issueB(0, 0);
    unsigned a_voff[6];
#pragma unroll
    for (int e = 0; e < 6; ++e) {
      const int ii = (L + 4 * e) < 22 ? L + 4 * e : L + 16;
      const int row = 8 * ii + (lane >> 3);
      int tok = tok0 + row;
      tok = tok < 0 ? 0 : (tok > S_ - 1 ? S_ - 1 : tok);
      a_voff[e] = (unsigned)(tok * 1280 + (((lane & 7) ^ ((row >> 1) & 7)) << 4));
    }
    const unsigned a_dst5 = L < 2 ? 20480u : 16384u;
    const bool a_on0 = L != 0 || lane >= 8;
    auto issueA = [&](int j, unsigned slot_off) {
      const char* ab = (const char*)x + j * 128;
      const unsigned dst = LDS_A + slot_off + L * 1024;
      if (a_on0) dma16(ab, a_voff[0], dst);
#pragma unroll
      for (int e = 1; e < 5; ++e) dma16(ab, a_voff[e], dst + e * 4096);
      dma16(ab, a_voff[5], dst + a_dst5);
    };
    issueA(0, 0); issueB(1, B_SLOT); issueA(1, A_SLOT); issueB(2, 2 * B_SLOT);
    issueB(3, 3 * B_SLOT); issueA(2, 2 * A_SLOT); issueB(4, 4 * B_SLOT);
    if (tid - 512 < 24) {
      u32x4 ones = {0x3F803F80u, 0x3F803F80u, 0x3F803F80u, 0x3F803F80u};
      *LDSP(u32x4, LDS_A + ((tid - 512) >> 3) * A_SLOT + (tid & 7) * 16) = ones;
    }
    float ssum[6], ssq[6];
#pragma unroll
    for (int i = 0; i < 6; ++i) { ssum[i] = 0.f; ssq[i] = 0.f; }
    const unsigned s_rd = LDS_A + L * 44 * 128 + lane * 16;
    auto stats = [&](int i0, unsigned ao) {
      u32x4 sd[3];
#pragma unroll
      for (int i = 0; i < 3; ++i) sd[i] = *LDSP(u32x4, s_rd + ao + (i0 + i) * 1024);
#pragma unroll
      for (int i = 0; i < 3; ++i)
#pragma unroll
        for (int j = 0; j < 4; ++j) {
          const unsigned v = sd[i][j];
          const float lo = __builtin_bit_cast(float, v << 16), hi = __builtin_bit_cast(float, v & 0xFFFF0000u);
          ssum[i0 + i] += lo; ssq[i0 + i] = __builtin_fmaf(lo, lo, ssq[i0 + i]);
          ssum[i0 + i] += hi; ssq[i0 + i] = __builtin_fmaf(hi, hi, ssq[i0 + i]);
        }
    };
    wait_vm<28>();
    wait_lgkm0();
    __builtin_amdgcn_s_barrier();
    unsigned bo = 0, ao = 0, aop = 2 * A_SLOT;
#pragma unroll 1
    for (int t = 0; t < 20; t += 2) {
      const unsigned bo1 = bo == 4 * B_SLOT ? 0 : bo + B_SLOT;
      const unsigned bo2 = bo1 == 4 * B_SLOT ? 0 : bo1 + B_SLOT;
      if (t == 0) wait_vm<24>(); else if (t <= 14) wait_vm<18>(); else if (t == 16) wait_vm<14>(); else wait_vm<6>();
      wait_lgkm0();
      __builtin_amdgcn_s_barrier();
      if (t >= 2 && t <= 14) issueA((t >> 1) + 2, aop);
      if (t == 16) {
        const int jb = job < 136 ? job : 0;
        const int ks = jb >> 1, half = jb & 1;
        const char* wb = ks < 20 ? (const char*)wo + (size_t)ks * 32 * 1280 : (const char*)wd + (size_t)(ks - 20) * 32 * 1280;
#pragma unroll
        for (int e = 0; e < 6; ++e) {
          const int pe = e < 5 ? e : 4;
          const int o = 1024 * (L + 4 * pe) + 16 * lane;
          const int row = o / 640;
          dma16(wb, (unsigned)(row * 1280 + 640 * half + (o - row * 640)), LDS_A + A_SLOT + (L + 4 * pe) * 1024);
        }
      }
      if (t <= 14) issueB(t + 5, bo);
      stats(0, ao);
      if (t == 0) wait_vm<18>(); else if (t <= 12) wait_vm<18>(); else if (t == 14) wait_vm<18>(); else if (t == 16) wait_vm<10>(); else wait_vm<0>();
      wait_lgkm0();
      __builtin_amdgcn_s_barrier();
      if (t <= 12) issueB(t + 6, bo1);
      stats(3, ao);
      bo = bo2; aop = ao; ao = ao == 2 * A_SLOT ? 0 : ao + A_SLOT;
    }
#pragma unroll
    for (int i = 0; i < 6; ++i) {
      float a = ssum[i], b = ssq[i];
      a += __shfl_xor(a, 1); b += __shfl_xor(b, 1);
      a += __shfl_xor(a, 2); b += __shfl_xor(b, 2);
      a += __shfl_xor(a, 4); b += __shfl_xor(b, 4);
      if ((lane & 7) == 0 && 64 * i + lane < 352) {
        const float mu = a * (1.0f / D_);
        float var = b * (1.0f / D_) - mu * mu;
        var = var < 0.f ? 0.f : var;
        const float rs = __builtin_amdgcn_rsqf(var + 1e-5f);
        *LDSP(f32x2, LDS_ST + (L * 44 + 8 * i + (lane >> 3)) * 8) = f32x2{rs, -mu * rs};
      }
    }
#pragma unroll
    for (int m = 0; m < 6; ++m)
#pragma unroll
      for (int n = 0; n < 4; ++n) acc[m][n] = f32x4{0.f, 0.f, 0.f, 0.f};
  } else {
    unsigned a_rd[2];
#pragma unroll
    for (int s = 0; s < 2; ++s)
      a_rd[s] = LDS_A + mh * 6 * 2048 + c * 128 + (((4 * s + q) ^ ((c >> 1) & 7)) << 4);
    unsigned b_rd[4];
    {
      const int a = c >> 2, p = c & 3;
      const int hh = a | ((q & 1) << 2);
#pragma unroll
      for (int n = 0; n < 4; ++n)
        b_rd[n] = LDS_B + (ntile[n] >> 3) * 8192 + (8 * q + a) * 256 + (((ntile[n] & 7) ^ hh) << 5) + 8 * p;
    }
#pragma unroll
    for (int m = 0; m < 6; ++m)
#pragma unroll
      for (int n = 0; n < 4; ++n) {
        acc[m][n] = f32x4{0.f, 0.f, 0.f, 0.f};
        if (m < 5) asm volatile("" : "+a"(acc[m][n])); else asm volatile("s_nop 1" : "+v"(acc[m][n]));
      }
    bf16x8 af[3], bfr[2][4];
    auto step = [&](auto par, unsigned aoc, unsigned aon, unsigned bon) {
      constexpr int P = decltype(par)::value;
      wait_lgkm0();
      __builtin_amdgcn_s_barrier();
      SB();
      static_for<0, 6>([&](auto mc) {
        constexpr int m = decltype(mc)::value;
        if constexpr (m < 4) af[(m + 2) % 3] = *LDSP(bf16x8, a_rd[P] + aoc + (m + 2) * 2048);
        else af[(m + 2) % 3] = *LDSP(bf16x8, a_rd[1 - P] + aon + (m - 4) * 2048);
        if constexpr (m < 4) bfr[1 - P][m] = tr_frag(b_rd[m] + bon, b_rd[m] + bon + 1024);
        SB();
        if (m < 5 || mh == 0) {
#pragma unroll
          for (int n = 0; n < 4; ++n) {
            if constexpr (m < 5) mfma_acc(acc[m][n], bfr[P][n], af[m % 3]);
            else mfma_accv(acc[m][n], bfr[P][n], af[m % 3]);
          }
        }
        SB();
      });
    };
    using P0 = std::integral_constant<int, 0>;
    using P1 = std::integral_constant<int, 1>;
    __builtin_amdgcn_s_barrier();
#pragma unroll
    for (int n = 0; n < 4; ++n) bfr[0][n] = tr_frag(b_rd[n], b_rd[n] + 1024);
    af[0] = *LDSP(bf16x8, a_rd[0]);
    af[1] = *LDSP(bf16x8, a_rd[0] + 2048);
    STAMP(1);
    unsigned bo = 0, ao = 0;
#pragma unroll 1
    for (int t = 0; t < 20; t += 2) {
      const unsigned bo1 = bo == 4 * B_SLOT ? 0 : bo + B_SLOT;
      const unsigned bo2 = bo1 == 4 * B_SLOT ? 0 : bo1 + B_SLOT;
      const unsigned ao1 = ao == 2 * A_SLOT ? 0 : ao + A_SLOT;
      step(P0{}, ao, ao, bo1);
      step(P1{}, ao, ao1, bo2);
      bo = bo2; ao = ao1;
    }
    asm volatile("s_nop 15\n\ts_nop 15" ::: "memory");
    STAMP(6);
    if (mh == 0 && c == 0) {
#pragma unroll
      for (int n = 0; n < 4; ++n) *LDSP(f32x4, LDS_CS + (ntile[n] * 16 + 4 * q) * 4) = acc[0][n];
    }
  }
  wait_lgkm0();
  __builtin_amdgcn_s_barrier();

  f32x4 cs[4];
#pragma unroll
  for (int n = 0; n < 4; ++n) cs[n] = *LDSP(f32x4, LDS_CS + (ntile[n] * 16 + 4 * q) * 4);
  float rstd[6], nmr[6];
#pragma unroll
  for (int m = 0; m < 6; ++m) {
    const f32x2 v = *LDSP(f32x2, LDS_ST + ((6 * mh + m) * 16 + c) * 8);
    rstd[m] = v[0]; nmr[m] = v[1];
  }

  char* a2 = ws + WS_A2;
  STAMP(7);
  if (!is_att) {
    const int ks = 20 + 4 * cbk + wq;
    constexpr float NL2E = -1.4426950408889634f;
    if (wave < 8) {
#pragma unroll
    for (int m = 0; m < 6; ++m) {
      if (m < 5 || mh == 0) {
        const int trow = (6 * mh + m) * 16 + c;
        const int tok = tok0 + trow;
        const float rg = rstd[m] * NL2E, ng = nmr[m] * NL2E;
        float hv[8];
#pragma unroll
        for (int n = 0; n < 2; ++n)
#pragma unroll
          for (int r = 0; r < 4; ++r) {
            const float e = __builtin_amdgcn_exp2f(__builtin_fmaf(acc[m][n][r], rg, ng * cs[n][r]));
            const float u = __builtin_fmaf(acc[m][n + 2][r], rstd[m], nmr[m] * cs[n + 2][r]);
            hv[4 * n + r] = u * __builtin_amdgcn_rcpf(1.0f + e);
          }
        u32x4 pk = {pack2(hv[0], hv[1]), pack2(hv[2], hv[3]), pack2(hv[4], hv[5]), pack2(hv[6], hv[7])};
        if (trow >= 1 && tok < S_)
          *(u32x4*)(a2 + (size_t)(((tok >> 6) * NKS2 + ks) * 4 + ((tok >> 4) & 3)) * 1024 + (16 * q + (tok & 15)) * 16) = pk;
      }
    }
    } else if (job < 136) {
      const int ks2 = job >> 1, half = job & 1;
      const int a = c >> 2, p = c & 3;
#pragma unroll 1
      for (int u = wq; u < 20; u += 4) {
        const int cl = u / 5, n5 = u - 5 * cl;
        const int colst = n5 < 4 ? 32 * (n5 >> 1) + 8 * p + 4 * (n5 & 1) : 64 + 4 * p;
        const unsigned ad = LDS_A + 1 * A_SLOT + (4 * q + a) * 640 + (80 * cl + colst) * 2;
        bf16x8 f = tr_frag(ad, ad + 16 * 640);
        const int cb2 = 4 * half + cl;
        *(bf16x8*)(ws + WS_W2 + (size_t)((cb2 * NKS2 + ks2) * 5 + n5) * 1024 + lane * 16) = f;
      }
    }
  } else {
#pragma unroll
    for (int m = 0; m < 6; ++m) {
      if (wave < 8 && (m < 5 || mh == 0)) {
        const int trow = (6 * mh + m) * 16 + c;
#pragma unroll
        for (int n = 0; n < 4; ++n) {
          float y[4];
#pragma unroll
          for (int r = 0; r < 4; ++r) y[r] = __builtin_fmaf(acc[m][n][r], rstd[m], nmr[m] * cs[n][r]);
          if (n < 3 || wq < 3)
            *LDSP(u32x2, trow * QROW + (ntile[n] * 16 + 4 * q) * 2) = u32x2{pack2(y[0], y[1]), pack2(y[2], y[3])};
        }
      }
    }
    float bias[8];
#pragma unroll
    for (int j = 0; j < 8; ++j) {
      const int diff = 16 + c - (16 * (j >> 2) + 4 * q + (j & 3));
      bias[j] = (diff >= 0 && diff <= 14 && (diff & 1) == 0) ? 0.f : -3.0e38f;
    }
    const float sl2 = 0.22360679774997896f * 1.4426950408889634f;
    const int dofs = q < 2 ? 16 * q : 32;
    const unsigned qk_l = c * QROW + dofs;
    const unsigned v_l = (4 * q + (c >> 2)) * QROW + 320 + 8 * (c & 3);
    wait_lgkm0();
    __builtin_amdgcn_s_barrier();
    STAMP(8);
#pragma unroll 1
    for (int unit = wave; unit < 40; unit += 12) {
      const int mq = 1 + (unit >> 2), hh = unit & 3;
      const unsigned ub = 16 * (mq - 1) * QROW + 40 * hh;
      u32x2 q0 = *LDSP(u32x2, ub + qk_l + 16 * QROW), q1 = *LDSP(u32x2, ub + qk_l + 16 * QROW + 8);
      u32x2 k00 = *LDSP(u32x2, ub + qk_l + 160), k01 = *LDSP(u32x2, ub + qk_l + 168);
      u32x2 k10 = *LDSP(u32x2, ub + qk_l + 16 * QROW + 160), k11 = *LDSP(u32x2, ub + qk_l + 16 * QROW + 168);
      const bf16x8 vf0 = tr_frag(ub + v_l, ub + v_l + 16 * QROW);
      const bf16x8 vf1 = tr_frag(ub + v_l + 32, ub + v_l + 32 + 16 * QROW);
      if (q >= 2) { q1 = u32x2{0u, 0u}; }
      if (q == 3) { q0 = u32x2{0u, 0u}; }
      const bf16x8 qf = __builtin_bit_cast(bf16x8, u32x4{q0[0], q0[1], q1[0], q1[1]});
      const f32x4 z4 = {0.f, 0.f, 0.f, 0.f};
      const f32x4 s0 = __builtin_amdgcn_mfma_f32_16x16x32_bf16(__builtin_bit_cast(bf16x8, u32x4{k00[0], k00[1], k01[0], k01[1]}), qf, z4, 0, 0, 0);
      const f32x4 s1 = __builtin_amdgcn_mfma_f32_16x16x32_bf16(__builtin_bit_cast(bf16x8, u32x4{k10[0], k10[1], k11[0], k11[1]}), qf, z4, 0, 0, 0);
      float lg[8];
#pragma unroll
      for (int j = 0; j < 4; ++j) {
        lg[j] = __builtin_fmaf(s0[j], sl2, bias[j]);
        lg[4 + j] = __builtin_fmaf(s1[j], sl2, bias[4 + j]);
      }
      if (tok0 + 16 * (mq - 1) < 0) {
#pragma unroll
        for (int j = 0; j < 4; ++j) lg[j] = -3.0e38f;
      }
      float mx = fmaxf(fmaxf(fmaxf(lg[0], lg[1]), fmaxf(lg[2], lg[3])), fmaxf(fmaxf(lg[4], lg[5]), fmaxf(lg[6], lg[7])));
      mx = red_max4(mx);
      float sum = 0.f;
#pragma unroll
      for (int j = 0; j < 8; ++j) { lg[j] = __builtin_amdgcn_exp2f(lg[j] - mx); sum += lg[j]; }
      const float inv = __builtin_amdgcn_rcpf(red_sum4(sum));
      const bf16x8 pf = __builtin_bit_cast(bf16x8, u32x4{pack2(lg[0] * inv, lg[1] * inv), pack2(lg[2] * inv, lg[3] * inv),
                                                         pack2(lg[4] * inv, lg[5] * inv), pack2(lg[6] * inv, lg[7] * inv)});
      const f32x4 o0 = __builtin_amdgcn_mfma_f32_16x16x32_bf16(vf0, pf, z4, 0, 0, 0);
      const f32x4 o1 = __builtin_amdgcn_mfma_f32_16x16x32_bf16(vf1, pf, z4, 0, 0, 0);
      const int tokb = tok0 + 16 * mq;
      if (tokb < S_) {
        char* ob = a2 + (size_t)((tokb >> 6) * NKS2 * 4 + ((tokb >> 4) & 3)) * 1024 + c * 16;
        const int col0 = 80 * hg + 20 * hh + 4 * q;
        *(u32x2*)(ob + (col0 >> 5) * 4096 + ((col0 >> 2) & 3) * 256 + ((col0 >> 4) & 1) * 8) = u32x2{pack2(o0[0], o0[1]), pack2(o0[2], o0[3])};
        const int col1 = 80 * hg + 20 * hh + 16;
        if (q == 0)
          *(u32x2*)(ob + (col1 >> 5) * 4096 + ((col1 >> 2) & 3) * 256 + ((col1 >> 4) & 1) * 8) = u32x2{pack2(o1[0], o1[1]), pack2(o1[2], o1[3])};
      }
    }
  }
  STAMP(9);
  STAMP(10);
  STAMP_OUT(0);
}

constexpr int K2_DEPTH = 6;
__global__ __launch_bounds__(256, 1) void k2_kernel(const __hip_bfloat16* __restrict__ x,
                                                    char* __restrict__ ws,
                                                    __hip_bfloat16* __restrict__ out) {
  const int tid = threadIdx.x, lane = tid & 63;
  const int wave = __builtin_amdgcn_readfirstlane(tid >> 6);
  const int q = lane >> 4, c = lane & 15;
  const int xcd = blockIdx.x & 7, idx = blockIdx.x >> 3;
  const int rb = (xcd >> 1) * 8 + (idx >> 2), cb = (xcd & 1) * 4 + (idx & 3);
  STAMP_DECL;
  STAMP(0);

  const char* ap = ws + WS_A2 + (size_t)(rb * NKS2 + 17 * wave) * 4096 + lane * 16;
  const char* wp = ws + WS_W2 + (size_t)(cb * NKS2 + 17 * wave) * 5120 + lane * 16;

  bf16x8 fa[K2_DEPTH][4], fw[K2_DEPTH][5];
  f32x4 acc[4][5];
#pragma unroll
  for (int m = 0; m < 4; ++m)
#pragma unroll
    for (int n = 0; n < 5; ++n) acc[m][n] = f32x4{0.f, 0.f, 0.f, 0.f};

  auto ld = [&](int i) {
    const int s = i % K2_DEPTH;
#pragma unroll
    for (int m = 0; m < 4; ++m) fa[s][m] = *(const bf16x8*)(ap + i * 4096 + m * 1024);
#pragma unroll
    for (int n = 0; n < 5; ++n) fw[s][n] = *(const bf16x8*)(wp + i * 5120 + n * 1024);
  };
#pragma unroll
  for (int i = 0; i < K2_DEPTH; ++i) ld(i);
  __builtin_amdgcn_sched_barrier(0);
#pragma unroll
  for (int i = 0; i < 17; ++i) {
    const int s = i % K2_DEPTH;
#pragma unroll
    for (int m = 0; m < 4; ++m)
#pragma unroll
      for (int n = 0; n < 5; ++n)
        acc[m][n] = __builtin_amdgcn_mfma_f32_16x16x32_bf16(fw[s][n], fa[s][m], acc[m][n], 0, 0, 0);
    __builtin_amdgcn_sched_barrier(0);
    if (i + K2_DEPTH < 17) ld(i + K2_DEPTH);
    __builtin_amdgcn_sched_barrier(0);
  }

  const int tok = rb * 64 + wave * 16 + c;
  const size_t obase = (size_t)tok * D_ + cb * 80;
  const u32x4 x0 = *(const u32x4*)(x + obase + 8 * q);
  const u32x4 x1 = *(const u32x4*)(x + obase + 32 + 8 * q);
  const u32x2 x2 = *(const u32x2*)(x + obase + 64 + 4 * q);

#pragma unroll
  for (int m = 0; m < 4; ++m) {
    if (m != wave) {
      const int si = wave < m ? wave : wave - 1;
#pragma unroll
      for (int n = 0; n < 5; ++n) *LDSP(f32x4, ((m * 3 + si) * 5 + n) * 1024 + lane * 16) = acc[m][n];
    }
  }
  __syncthreads();
  f32x4 r[5];
#pragma unroll
  for (int n = 0; n < 5; ++n) {
    r[n] = wave == 0 ? acc[0][n] : (wave == 1 ? acc[1][n] : (wave == 2 ? acc[2][n] : acc[3][n]));
#pragma unroll
    for (int si = 0; si < 3; ++si) r[n] += *LDSP(f32x4, ((wave * 3 + si) * 5 + n) * 1024 + lane * 16);
  }
  auto lo = [](unsigned v) { return __builtin_bit_cast(float, v << 16); };
  auto hi = [](unsigned v) { return __builtin_bit_cast(float, v & 0xFFFF0000u); };
  u32x4 o0 = {pack2(r[0][0] + lo(x0[0]), r[0][1] + hi(x0[0])), pack2(r[0][2] + lo(x0[1]), r[0][3] + hi(x0[1])),
              pack2(r[1][0] + lo(x0[2]), r[1][1] + hi(x0[2])), pack2(r[1][2] + lo(x0[3]), r[1][3] + hi(x0[3]))};
  u32x4 o1 = {pack2(r[2][0] + lo(x1[0]), r[2][1] + hi(x1[0])), pack2(r[2][2] + lo(x1[1]), r[2][3] + hi(x1[1])),
              pack2(r[3][0] + lo(x1[2]), r[3][1] + hi(x1[2])), pack2(r[3][2] + lo(x1[3]), r[3][3] + hi(x1[3]))};
  u32x2 o2 = {pack2(r[4][0] + lo(x2[0]), r[4][1] + hi(x2[0])), pack2(r[4][2] + lo(x2[1]), r[4][3] + hi(x2[1]))};
  *(u32x4*)(out + obase + 8 * q) = o0;
  *(u32x4*)(out + obase + 32 + 8 * q) = o1;
  *(u32x2*)(out + obase + 64 + 4 * q) = o2;
  STAMP(6);
  STAMP_OUT(1);
}

extern "C" void kernel_launch(void* const* d_in, const int* in_sizes, int n_in,
                              void* d_out, int out_size, void* d_ws, size_t ws_size,
                              hipStream_t stream) {
  const __hip_bfloat16* x = (const __hip_bfloat16*)d_in[0];
  const __hip_bfloat16* wqkv = (const __hip_bfloat16*)d_in[1];
  const __hip_bfloat16* wo = (const __hip_bfloat16*)d_in[2];
  const __hip_bfloat16* wg = (const __hip_bfloat16*)d_in[4];
  const __hip_bfloat16* wu = (const __hip_bfloat16*)d_in[5];
  const __hip_bfloat16* wd = (const __hip_bfloat16*)d_in[6];
  (void)hipFuncSetAttribute((const void*)k1_kernel, hipFuncAttributeMaxDynamicSharedMemorySize, LDS_K1);
  k1_kernel<<<256, 768, LDS_K1, stream>>>(x, wqkv, wo, wg, wu, wd, (char*)d_ws);
  k2_kernel<<<256, 256, 61440, stream>>>(x, (char*)d_ws, (__hip_bfloat16*)d_out);
}
```

```cpp
#include <hip/hip_runtime.h>
#include <hip/hip_bf16.h>
#include <type_traits>

typedef short s16x4 __attribute__((ext_vector_type(4)));
typedef short s16x8 __attribute__((ext_vector_type(8)));
typedef __bf16 bf16x2 __attribute__((ext_vector_type(2)));
typedef __bf16 bf16x8 __attribute__((ext_vector_type(8)));
typedef float f32x2 __attribute__((ext_vector_type(2)));
typedef float f32x4 __attribute__((ext_vector_type(4)));
typedef unsigned u32x2 __attribute__((ext_vector_type(2)));
typedef unsigned u32x4 __attribute__((ext_vector_type(4)));
#define LDSP(T, a) ((__attribute__((address_space(3))) T*)(size_t)(unsigned)(a))

constexpr int S_ = 2048, D_ = 640, F_ = 1536;
constexpr int NKS2 = 68;
constexpr size_t WS_A2 = 0;
constexpr size_t WS_W2 = 9437184;

constexpr int A_SLOT = 22528;
constexpr int LDS_A = 0;
constexpr int B_SLOT = 16384;
constexpr int LDS_B = 3 * A_SLOT;
constexpr int LDS_ST = LDS_B + 5 * B_SLOT;
constexpr int LDS_CS = LDS_ST + 4 * 176 * 8;
constexpr int LDS_DUMMY = LDS_CS + 1024;
constexpr int LDS_K1 = LDS_DUMMY + 1024;
constexpr int QROW = 488;

__device__ __forceinline__ void dma16(const void* sbase, unsigned voff, unsigned lds_addr) {
  unsigned keep;
  asm volatile("s_mov_b32 %0, m0\n\ts_mov_b32 m0, %3\n\ts_nop 0\n\tglobal_load_lds_dwordx4 %1, %2\n\ts_mov_b32 m0, %0"
               : "=&s"(keep) : "v"(voff), "s"(sbase), "s"(lds_addr) : "memory");
}
template <int N> __device__ __forceinline__ void wait_vm() { asm volatile("s_waitcnt vmcnt(%0)" ::"i"(N) : "memory"); }
__device__ __forceinline__ void wait_lgkm0() {
  __builtin_amdgcn_s_waitcnt(0xC07F);
  asm volatile("" ::: "memory");
}

__device__ __forceinline__ bf16x8 tr_frag(unsigned a0, unsigned a1) {
  s16x4 lo = __builtin_amdgcn_ds_read_tr16_b64_v4i16(LDSP(s16x4, a0));
  s16x4 hi = __builtin_amdgcn_ds_read_tr16_b64_v4i16(LDSP(s16x4, a1));
  s16x8 v = __builtin_shufflevector(lo, hi, 0, 1, 2, 3, 4, 5, 6, 7);
  return __builtin_bit_cast(bf16x8, v);
}
__device__ __forceinline__ void mfma_acc(f32x4& c, const bf16x8& a, const bf16x8& b) {
  asm volatile("v_mfma_f32_16x16x32_bf16 %0, %1, %2, %0" : "+a"(c) : "v"(a), "v"(b));
}
__device__ __forceinline__ void mfma_accv(f32x4& c, const bf16x8& a, const bf16x8& b) {
  asm volatile("v_mfma_f32_16x16x32_bf16 %0, %1, %2, %0" : "+v"(c) : "v"(a), "v"(b));
}
__device__ __forceinline__ void swap16(float& a, float& b) {
  asm volatile("s_nop 1\n\tv_permlane16_swap_b32 %0, %1" : "+v"(a), "+v"(b));
}
__device__ __forceinline__ void swap32(float& a, float& b) {
  asm volatile("s_nop 1\n\tv_permlane32_swap_b32 %0, %1" : "+v"(a), "+v"(b));
}
__device__ __forceinline__ float red_max4(float a) {
  float b = a;
  swap16(a, b); a = fmaxf(a, b); b = a;
  swap32(a, b); return fmaxf(a, b);
}
__device__ __forceinline__ float red_sum4(float a) {
  float b = a;
  swap16(a, b); a = a + b; b = a;
  swap32(a, b); return a + b;
}
__device__ __forceinline__ unsigned pack2(float a, float b) {
  bf16x2 p = {(__bf16)a, (__bf16)b};
  return __builtin_bit_cast(unsigned, p);
}

#define STAMP(i)
#define STAMP_DECL
#define STAMP_OUT(k)
template <int I, int N, class Fn> __device__ __forceinline__ void static_for(Fn&& f) {
  if constexpr (I < N) { f(std::integral_constant<int, I>{}); static_for<I + 1, N>(f); }
}

#define SB() __builtin_amdgcn_sched_barrier(0)
#define PC_BACK 0

__global__ __launch_bounds__(768, 3) void k1_kernel(const __hip_bfloat16* __restrict__ x,
                                                    const __hip_bfloat16* __restrict__ wqkv,
                                                    const __hip_bfloat16* __restrict__ wo,
                                                    const __hip_bfloat16* __restrict__ wg,
                                                    const __hip_bfloat16* __restrict__ wu,
                                                    const __hip_bfloat16* __restrict__ wd,
                                                    char* __restrict__ ws) {
  STAMP_DECL;
  STAMP(0);
  const int tid = threadIdx.x, lane = tid & 63;
  const int wave = __builtin_amdgcn_readfirstlane(tid >> 6);
  const int mh = (wave >> 2) & 1, wq = wave & 3;
  const int q = lane >> 4, c = lane & 15;

  const int xcd = blockIdx.x & 7, idx = blockIdx.x >> 3;
  const int xr = xcd >> 2, xc = xcd & 3;
  const int natt = xr == 0 ? 14 : 12;
  const bool is_att = idx < natt;
  int tok0, cbk = 0, hg = 0, job = 1000;
  if (is_att) {
    const int ab = (xr == 0 ? 0 : 7) + (idx >> 1);
    hg = 2 * xc + (idx & 1);
    tok0 = 160 * ab - 16;
  } else {
    const int j = idx - natt;
    if (j >= 18) return;
    const int rb = 6 * xr + j / 3;
    cbk = 3 * xc + j % 3;
    tok0 = 175 * rb - 1;
    job = rb * 12 + cbk;
  }
  int ntile[4];
#pragma unroll
  for (int n = 0; n < 4; ++n) ntile[n] = is_att ? 4 * wq + n : (n < 2 ? 2 * wq + n : 8 + 2 * wq + n - 2);
  f32x4 acc[6][4];

  if (wave >= 8) {
    const int L = wq;
    unsigned b_voff[4];
    const char* b_base[2];
    int b_step;
#pragma unroll
    for (int j = 0; j < 4; ++j) {
      const int row = 4 * (L + 4 * (j & 1)) + (lane >> 4);
      const int hsw = (row & 3) | (((row >> 3) & 1) << 2);
      const int g = (lane & 15) ^ (hsw << 1);
      if (is_att) {
        int Lc = 16 * (j >> 1) + g;
        Lc = Lc > 29 ? 29 : Lc;
        const int sec = Lc / 10;
        b_voff[j] = (unsigned)(row * 3840 + (640 * sec + 80 * hg + 8 * (Lc - 10 * sec)) * 2);
      } else {
        b_voff[j] = (unsigned)(row * 3072 + (128 * cbk + 8 * g) * 2);
      }
    }
    if (is_att) { b_base[0] = (const char*)wqkv; b_base[1] = (const char*)wqkv; b_step = 32 * 3840; }
    else { b_base[0] = (const char*)wg; b_base[1] = (const char*)wu; b_step = 32 * 3072; }
    auto issueB = [&](int j, unsigned slot_off) {
#pragma unroll
      for (int e = 0; e < 4; ++e)
        dma16(b_base[e >> 1] + (size_t)j * b_step, b_voff[e], LDS_B + slot_off + (L + 4 * e) * 1024);
    };
    issueB(0, 0);
    unsigned a_voff[6];
#pragma unroll
    for (int e = 0; e < 6; ++e) {
      const int ii = (L + 4 * e) < 22 ? L + 4 * e : L + 16;
      const int row = 8 * ii + (lane >> 3);
      int tok = tok0 + row;
      tok = tok < 0 ? 0 : (tok > S_ - 1 ? S_ - 1 : tok);
      a_voff[e] = (unsigned)(tok * 1280 + (((lane & 7) ^ ((row >> 1) & 7)) << 4));
    }
    const unsigned a_dst5 = L < 2 ? 20480u : 16384u;
    const bool a_on0 = L != 0 || lane >= 8;
    auto issueA = [&](int j, unsigned slot_off) {
      const char* ab = (const char*)x + j * 128;
      const unsigned dst = LDS_A + slot_off + L * 1024;
      if (a_on0) dma16(ab, a_voff[0], dst);
#pragma unroll
      for (int e = 1; e < 5; ++e) dma16(ab, a_voff[e], dst + e * 4096);
      dma16(ab, a_voff[5], dst + a_dst5);
    };
    issueA(0, 0); issueB(1, B_SLOT); issueA(1, A_SLOT); issueB(2, 2 * B_SLOT);
    issueB(3, 3 * B_SLOT); issueA(2, 2 * A_SLOT); issueB(4, 4 * B_SLOT);
    if (tid - 512 < 24) {
      u32x4 ones = {0x3F803F80u, 0x3F803F80u, 0x3F803F80u, 0x3F803F80u};
      *LDSP(u32x4, LDS_A + ((tid - 512) >> 3) * A_SLOT + (tid & 7) * 16) = ones;
    }
    f32x4 gq[3], gs[3];
#pragma unroll
    for (int i = 0; i < 3; ++i) { gq[i] = f32x4{0.f, 0.f, 0.f, 0.f}; gs[i] = f32x4{0.f, 0.f, 0.f, 0.f}; }
    const bf16x8 ones8 = __builtin_bit_cast(bf16x8, u32x4{0x3F803F80u, 0x3F803F80u, 0x3F803F80u, 0x3F803F80u});
    const unsigned s_rd = LDS_A + L * 2048 + c * 128;
    auto stats = [&](int sub, unsigned ao) {
      const unsigned ad = s_rd + ao + (((4 * sub + q) ^ ((c >> 1) & 7)) << 4);
      bf16x8 f[3];
#pragma unroll
      for (int i = 0; i < 3; ++i) f[i] = *LDSP(bf16x8, ad + i * 8192);
#pragma unroll
      for (int i = 0; i < 3; ++i) {
        gq[i] = __builtin_amdgcn_mfma_f32_16x16x32_bf16(f[i], f[i], gq[i], 0, 0, 0);
        gs[i] = __builtin_amdgcn_mfma_f32_16x16x32_bf16(ones8, f[i], gs[i], 0, 0, 0);
      }
    };
    wait_vm<28>();
    wait_lgkm0();
    __builtin_amdgcn_s_barrier();
    unsigned bo = 0, ao = 0, aop = 2 * A_SLOT;
#pragma unroll 1
    for (int t = 0; t < 20; t += 2) {
      const unsigned bo1 = bo == 4 * B_SLOT ? 0 : bo + B_SLOT;
      const unsigned bo2 = bo1 == 4 * B_SLOT ? 0 : bo1 + B_SLOT;
      if (t == 0) wait_vm<24>(); else if (t <= 14) wait_vm<18>(); else if (t == 16) wait_vm<14>(); else wait_vm<6>();
      wait_lgkm0();
      __builtin_amdgcn_s_barrier();
      if (t >= 2 && t <= 14) issueA((t >> 1) + 2, aop);
      if (t == 16) {
        const int jb = job < 136 ? job : 0;
        const int ks = jb >> 1, half = jb & 1;
        const char* wb = ks < 20 ? (const char*)wo + (size_t)ks * 32 * 1280 : (const char*)wd + (size_t)(ks - 20) * 32 * 1280;
#pragma unroll
        for (int e = 0; e < 6; ++e) {
          const int pe = e < 5 ? e : 4;
          const int o = 1024 * (L + 4 * pe) + 16 * lane;
          const int row = o / 640;
          dma16(wb, (unsigned)(row * 1280 + 640 * half + (o - row * 640)), LDS_A + A_SLOT + (L + 4 * pe) * 1024);
        }
      }
      if (t <= 14) issueB(t + 5, bo);
      stats(0, ao);
      if (t == 0) wait_vm<18>(); else if (t <= 12) wait_vm<18>(); else if (t == 14) wait_vm<18>(); else if (t == 16) wait_vm<10>(); else wait_vm<0>();
      wait_lgkm0();
      __builtin_amdgcn_s_barrier();
      if (t <= 12) issueB(t + 6, bo1);
      stats(1, ao);
      bo = bo2; aop = ao; ao = ao == 2 * A_SLOT ? 0 : ao + A_SLOT;
    }
#pragma unroll
    for (int i = 0; i < 3; ++i) {
      const int r = c & 3;
      const float b = r == 0 ? gq[i][0] : (r == 1 ? gq[i][1] : (r == 2 ? gq[i][2] : gq[i][3]));
      if (q == (c >> 2) && L + 4 * i < 11) {
        const float mu = gs[i][0] * (1.0f / D_);
        float var = b * (1.0f / D_) - mu * mu;
        var = var < 0.f ? 0.f : var;
        const float rs = __builtin_amdgcn_rsqf(var + 1e-5f);
        *LDSP(f32x2, LDS_ST + ((L + 4 * i) * 16 + c) * 8) = f32x2{rs, -mu * rs};
      }
    }
#pragma unroll
    for (int m = 0; m < 6; ++m)
#pragma unroll
      for (int n = 0; n < 4; ++n) acc[m][n] = f32x4{0.f, 0.f, 0.f, 0.f};
  } else {
    unsigned code_pf = 0;
    if (wave < 6) {
      const char* pc = (const char*)(__builtin_amdgcn_s_getpc() & ~255ull);
      code_pf = *(const unsigned*)(pc - PC_BACK + wave * 4096 + lane * 64);
    }
    unsigned a_rd[2];
#pragma unroll
    for (int s = 0; s < 2; ++s)
      a_rd[s] = LDS_A + mh * 6 * 2048 + c * 128 + (((4 * s + q) ^ ((c >> 1) & 7)) << 4);
    unsigned b_rd[4];
    {
      const int a = c >> 2, p = c & 3;
      const int hh = a | ((q & 1) << 2);
#pragma unroll
      for (int n = 0; n < 4; ++n)
        b_rd[n] = LDS_B + (ntile[n] >> 3) * 8192 + (8 * q + a) * 256 + (((ntile[n] & 7) ^ hh) << 5) + 8 * p;
    }
#pragma unroll
    for (int m = 0; m < 6; ++m)
#pragma unroll
      for (int n = 0; n < 4; ++n) {
        acc[m][n] = f32x4{0.f, 0.f, 0.f, 0.f};
        if (m < 5) asm volatile("" : "+a"(acc[m][n])); else asm volatile("s_nop 1" : "+v"(acc[m][n]));
      }
    bf16x8 af[6], bfr[2][4];
    auto step = [&](auto par, unsigned aon, unsigned bon) {
      constexpr int P = decltype(par)::value;
      __builtin_amdgcn_s_waitcnt(0xC27F);
      asm volatile("" ::: "memory");
      __builtin_amdgcn_s_barrier();
      SB();
      static_for<0, 6>([&](auto mc) {
        constexpr int m = decltype(mc)::value;
        if (m < 5 || mh == 0) {
#pragma unroll
          for (int n = 0; n < 4; ++n) {
            if constexpr (m < 5) mfma_acc(acc[m][n], bfr[P][n], af[m]);
            else mfma_accv(acc[m][n], bfr[P][n], af[m]);
          }
        }
        SB();
        af[m] = *LDSP(bf16x8, a_rd[1 - P] + aon + m * 2048);
        if constexpr (m < 4) bfr[1 - P][m] = tr_frag(b_rd[m] + bon, b_rd[m] + bon + 1024);
        SB();
      });
    };
    using P0 = std::integral_constant<int, 0>;
    using P1 = std::integral_constant<int, 1>;
    __builtin_amdgcn_s_barrier();
#pragma unroll
    for (int n = 0; n < 4; ++n) bfr[0][n] = tr_frag(b_rd[n], b_rd[n] + 1024);
#pragma unroll
    for (int m = 0; m < 6; ++m) af[m] = *LDSP(bf16x8, a_rd[0] + m * 2048);
    STAMP(1);
    unsigned bo = 0, ao = 0;
#pragma unroll 1
    for (int t = 0; t < 20; t += 2) {
      const unsigned bo1 = bo == 4 * B_SLOT ? 0 : bo + B_SLOT;
      const unsigned bo2 = bo1 == 4 * B_SLOT ? 0 : bo1 + B_SLOT;
      const unsigned ao1 = ao == 2 * A_SLOT ? 0 : ao + A_SLOT;
      step(P0{}, ao, bo1);
      step(P1{}, ao1, bo2);
      bo = bo2; ao = ao1;
    }
    asm volatile("s_nop 15\n\ts_nop 15" ::: "memory");
    asm volatile("" :: "v"(code_pf));
    STAMP(6);
    if (mh == 0 && c == 0) {
#pragma unroll
      for (int n = 0; n < 4; ++n) *LDSP(f32x4, LDS_CS + (ntile[n] * 16 + 4 * q) * 4) = acc[0][n];
    }
  }
  wait_lgkm0();
  __builtin_amdgcn_s_barrier();

  f32x4 cs[4];
#pragma unroll
  for (int n = 0; n < 4; ++n) cs[n] = *LDSP(f32x4, LDS_CS + (ntile[n] * 16 + 4 * q) * 4);
  float rstd[6], nmr[6];
#pragma unroll
  for (int m = 0; m < 6; ++m) {
    const f32x2 v = *LDSP(f32x2, LDS_ST + ((6 * mh + m) * 16 + c) * 8);
    rstd[m] = v[0]; nmr[m] = v[1];
  }

  char* a2 = ws + WS_A2;
  STAMP(7);
  if (!is_att) {
    const int ks = 20 + 4 * cbk + wq;
    constexpr float NL2E = -1.4426950408889634f;
    if (wave < 8) {
#pragma unroll
    for (int m = 0; m < 6; ++m) {
      if (m < 5 || mh == 0) {
        const int trow = (6 * mh + m) * 16 + c;
        const int tok = tok0 + trow;
        const float rg = rstd[m] * NL2E, ng = nmr[m] * NL2E;
        float hv[8];
#pragma unroll
        for (int n = 0; n < 2; ++n)
#pragma unroll
          for (int r = 0; r < 4; ++r) {
            const float e = __builtin_amdgcn_exp2f(__builtin_fmaf(acc[m][n][r], rg, ng * cs[n][r]));
            const float u = __builtin_fmaf(acc[m][n + 2][r], rstd[m], nmr[m] * cs[n + 2][r]);
            hv[4 * n + r] = u * __builtin_amdgcn_rcpf(1.0f + e);
          }
        u32x4 pk = {pack2(hv[0], hv[1]), pack2(hv[2], hv[3]), pack2(hv[4], hv[5]), pack2(hv[6], hv[7])};
        if (trow >= 1 && tok < S_)
          *(u32x4*)(a2 + (size_t)(((tok >> 6) * NKS2 + ks) * 4 + ((tok >> 4) & 3)) * 1024 + (16 * q + (tok & 15)) * 16) = pk;
      }
    }
    } else if (job < 136) {
      const int ks2 = job >> 1, half = job & 1;
      const int a = c >> 2, p = c & 3;
#pragma unroll 1
      for (int u = wq; u < 20; u += 4) {
        const int cl = u / 5, n5 = u - 5 * cl;
        const int colst = n5 < 4 ? 32 * (n5 >> 1) + 8 * p + 4 * (n5 & 1) : 64 + 4 * p;
        const unsigned ad = LDS_A + 1 * A_SLOT + (4 * q + a) * 640 + (80 * cl + colst) * 2;
        bf16x8 f = tr_frag(ad, ad + 16 * 640);
        const int cb2 = 4 * half + cl;
        *(bf16x8*)(ws + WS_W2 + (size_t)((cb2 * NKS2 + ks2) * 5 + n5) * 1024 + lane * 16) = f;
      }
    }
  } else {
#pragma unroll
    for (int m = 0; m < 6; ++m) {
      if (wave < 8 && (m < 5 || mh == 0)) {
        const int trow = (6 * mh + m) * 16 + c;
#pragma unroll
        for (int n = 0; n < 4; ++n) {
          float y[4];
#pragma unroll
          for (int r = 0; r < 4; ++r) y[r] = __builtin_fmaf(acc[m][n][r], rstd[m], nmr[m] * cs[n][r]);
          if (n < 3 || wq < 3)
            *LDSP(u32x2, trow * QROW + (ntile[n] * 16 + 4 * q) * 2) = u32x2{pack2(y[0], y[1]), pack2(y[2], y[3])};
        }
      }
    }
    float bias[8];
#pragma unroll
    for (int j = 0; j < 8; ++j) {
      const int diff = 16 + c - (16 * (j >> 2) + 4 * q + (j & 3));
      bias[j] = (diff >= 0 && diff <= 14 && (diff & 1) == 0) ? 0.f : -3.0e38f;
    }
    const float sl2 = 0.22360679774997896f * 1.4426950408889634f;
    const int dofs = q < 2 ? 16 * q : 32;
    const unsigned qk_l = c * QROW + dofs;
    const unsigned v_l = (4 * q + (c >> 2)) * QROW + 320 + 8 * (c & 3);
    wait_lgkm0();
    __builtin_amdgcn_s_barrier();
    STAMP(8);
#pragma unroll 1
    for (int unit = wave; unit < 40; unit += 12) {
      const int mq = 1 + (unit >> 2), hh = unit & 3;
      const unsigned ub = 16 * (mq - 1) * QROW + 40 * hh;
      u32x2 q0 = *LDSP(u32x2, ub + qk_l + 16 * QROW), q1 = *LDSP(u32x2, ub + qk_l + 16 * QROW + 8);
      u32x2 k00 = *LDSP(u32x2, ub + qk_l + 160), k01 = *LDSP(u32x2, ub + qk_l + 168);
      u32x2 k10 = *LDSP(u32x2, ub + qk_l + 16 * QROW + 160), k11 = *LDSP(u32x2, ub + qk_l + 16 * QROW + 168);
      const bf16x8 vf0 = tr_frag(ub + v_l, ub + v_l + 16 * QROW);
      const bf16x8 vf1 = tr_frag(ub + v_l + 32, ub + v_l + 32 + 16 * QROW);
      if (q >= 2) { q1 = u32x2{0u, 0u}; }
      if (q == 3) { q0 = u32x2{0u, 0u}; }
      const bf16x8 qf = __builtin_bit_cast(bf16x8, u32x4{q0[0], q0[1], q1[0], q1[1]});
      const f32x4 z4 = {0.f, 0.f, 0.f, 0.f};
      const f32x4 s0 = __builtin_amdgcn_mfma_f32_16x16x32_bf16(__builtin_bit_cast(bf16x8, u32x4{k00[0], k00[1], k01[0], k01[1]}), qf, z4, 0, 0, 0);
      const f32x4 s1 = __builtin_amdgcn_mfma_f32_16x16x32_bf16(__builtin_bit_cast(bf16x8, u32x4{k10[0], k10[1], k11[0], k11[1]}), qf, z4, 0, 0, 0);
      float lg[8];
#pragma unroll
      for (int j = 0; j < 4; ++j) {
        lg[j] = __builtin_fmaf(s0[j], sl2, bias[j]);
        lg[4 + j] = __builtin_fmaf(s1[j], sl2, bias[4 + j]);
      }
      if (tok0 + 16 * (mq - 1) < 0) {
#pragma unroll
        for (int j = 0; j < 4; ++j) lg[j] = -3.0e38f;
      }
      float mx = fmaxf(fmaxf(fmaxf(lg[0], lg[1]), fmaxf(lg[2], lg[3])), fmaxf(fmaxf(lg[4], lg[5]), fmaxf(lg[6], lg[7])));
      mx = red_max4(mx);
      float sum = 0.f;
#pragma unroll
      for (int j = 0; j < 8; ++j) { lg[j] = __builtin_amdgcn_exp2f(lg[j] - mx); sum += lg[j]; }
      const float inv = __builtin_amdgcn_rcpf(red_sum4(sum));
      const bf16x8 pf = __builtin_bit_cast(bf16x8, u32x4{pack2(lg[0] * inv, lg[1] * inv), pack2(lg[2] * inv, lg[3] * inv),
                                                         pack2(lg[4] * inv, lg[5] * inv), pack2(lg[6] * inv, lg[7] * inv)});
      const f32x4 o0 = __builtin_amdgcn_mfma_f32_16x16x32_bf16(vf0, pf, z4, 0, 0, 0);
      const f32x4 o1 = __builtin_amdgcn_mfma_f32_16x16x32_bf16(vf1, pf, z4, 0, 0, 0);
      const int tokb = tok0 + 16 * mq;
      if (tokb < S_) {
        char* ob = a2 + (size_t)((tokb >> 6) * NKS2 * 4 + ((tokb >> 4) & 3)) * 1024 + c * 16;
        const int col0 = 80 * hg + 20 * hh + 4 * q;
        *(u32x2*)(ob + (col0 >> 5) * 4096 + ((col0 >> 2) & 3) * 256 + ((col0 >> 4) & 1) * 8) = u32x2{pack2(o0[0], o0[1]), pack2(o0[2], o0[3])};
        const int col1 = 80 * hg + 20 * hh + 16;
        if (q == 0)
          *(u32x2*)(ob + (col1 >> 5) * 4096 + ((col1 >> 2) & 3) * 256 + ((col1 >> 4) & 1) * 8) = u32x2{pack2(o1[0], o1[1]), pack2(o1[2], o1[3])};
      }
    }
  }
  STAMP(9);
  STAMP(10);
  STAMP_OUT(0);
}

constexpr int K2_DEPTH = 6;
__global__ __launch_bounds__(256, 1) void k2_kernel(const __hip_bfloat16* __restrict__ x,
                                                    char* __restrict__ ws,
                                                    __hip_bfloat16* __restrict__ out) {
  const int tid = threadIdx.x, lane = tid & 63;
  const int wave = __builtin_amdgcn_readfirstlane(tid >> 6);
  const int q = lane >> 4, c = lane & 15;
  const int xcd = blockIdx.x & 7, idx = blockIdx.x >> 3;
  const int rb = (xcd >> 1) * 8 + (idx >> 2), cb = (xcd & 1) * 4 + (idx & 3);
  STAMP_DECL;
  STAMP(0);

  const char* ap = ws + WS_A2 + (size_t)(rb * NKS2 + 17 * wave) * 4096 + lane * 16;
  const char* wp = ws + WS_W2 + (size_t)(cb * NKS2 + 17 * wave) * 5120 + lane * 16;

  bf16x8 fa[K2_DEPTH][4], fw[K2_DEPTH][5];
  f32x4 acc[4][5];
#pragma unroll
  for (int m = 0; m < 4; ++m)
#pragma unroll
    for (int n = 0; n < 5; ++n) acc[m][n] = f32x4{0.f, 0.f, 0.f, 0.f};

  auto ld = [&](int i) {
    const int s = i % K2_DEPTH;
#pragma unroll
    for (int m = 0; m < 4; ++m) fa[s][m] = *(const bf16x8*)(ap + i * 4096 + m * 1024);
#pragma unroll
    for (int n = 0; n < 5; ++n) fw[s][n] = *(const bf16x8*)(wp + i * 5120 + n * 1024);
  };
#pragma unroll
  for (int i = 0; i < K2_DEPTH; ++i) ld(i);
  __builtin_amdgcn_sched_barrier(0);
#pragma unroll
  for (int i = 0; i < 17; ++i) {
    const int s = i % K2_DEPTH;
#pragma unroll
    for (int m = 0; m < 4; ++m)
#pragma unroll
      for (int n = 0; n < 5; ++n)
        acc[m][n] = __builtin_amdgcn_mfma_f32_16x16x32_bf16(fw[s][n], fa[s][m], acc[m][n], 0, 0, 0);
    __builtin_amdgcn_sched_barrier(0);
    if (i + K2_DEPTH < 17) ld(i + K2_DEPTH);
    __builtin_amdgcn_sched_barrier(0);
  }

  const int tok = rb * 64 + wave * 16 + c;
  const size_t obase = (size_t)tok * D_ + cb * 80;
  const u32x4 x0 = *(const u32x4*)(x + obase + 8 * q);
  const u32x4 x1 = *(const u32x4*)(x + obase + 32 + 8 * q);
  const u32x2 x2 = *(const u32x2*)(x + obase + 64 + 4 * q);

#pragma unroll
  for (int m = 0; m < 4; ++m) {
    if (m != wave) {
      const int si = wave < m ? wave : wave - 1;
#pragma unroll
      for (int n = 0; n < 5; ++n) *LDSP(f32x4, ((m * 3 + si) * 5 + n) * 1024 + lane * 16) = acc[m][n];
    }
  }
  __syncthreads();
  f32x4 r[5];
#pragma unroll
  for (int n = 0; n < 5; ++n) {
    r[n] = wave == 0 ? acc[0][n] : (wave == 1 ? acc[1][n] : (wave == 2 ? acc[2][n] : acc[3][n]));
#pragma unroll
    for (int si = 0; si < 3; ++si) r[n] += *LDSP(f32x4, ((wave * 3 + si) * 5 + n) * 1024 + lane * 16);
  }
  auto lo = [](unsigned v) { return __builtin_bit_cast(float, v << 16); };
  auto hi = [](unsigned v) { return __builtin_bit_cast(float, v & 0xFFFF0000u); };
  u32x4 o0 = {pack2(r[0][0] + lo(x0[0]), r[0][1] + hi(x0[0])), pack2(r[0][2] + lo(x0[1]), r[0][3] + hi(x0[1])),
              pack2(r[1][0] + lo(x0[2]), r[1][1] + hi(x0[2])), pack2(r[1][2] + lo(x0[3]), r[1][3] + hi(x0[3]))};
  u32x4 o1 = {pack2(r[2][0] + lo(x1[0]), r[2][1] + hi(x1[0])), pack2(r[2][2] + lo(x1[1]), r[2][3] + hi(x1[1])),
              pack2(r[3][0] + lo(x1[2]), r[3][1] + hi(x1[2])), pack2(r[3][2] + lo(x1[3]), r[3][3] + hi(x1[3]))};
  u32x2 o2 = {pack2(r[4][0] + lo(x2[0]), r[4][1] + hi(x2[0])), pack2(r[4][2] + lo(x2[1]), r[4][3] + hi(x2[1]))};
  *(u32x4*)(out + obase + 8 * q) = o0;
  *(u32x4*)(out + obase + 32 + 8 * q) = o1;
  *(u32x2*)(out + obase + 64 + 4 * q) = o2;
  STAMP(6);
  STAMP_OUT(1);
}

extern "C" void kernel_launch(void* const* d_in, const int* in_sizes, int n_in,
                              void* d_out, int out_size, void* d_ws, size_t ws_size,
                              hipStream_t stream) {
  const __hip_bfloat16* x = (const __hip_bfloat16*)d_in[0];
  const __hip_bfloat16* wqkv = (const __hip_bfloat16*)d_in[1];
  const __hip_bfloat16* wo = (const __hip_bfloat16*)d_in[2];
  const __hip_bfloat16* wg = (const __hip_bfloat16*)d_in[4];
  const __hip_bfloat16* wu = (const __hip_bfloat16*)d_in[5];
  const __hip_bfloat16* wd = (const __hip_bfloat16*)d_in[6];
  (void)hipFuncSetAttribute((const void*)k1_kernel, hipFuncAttributeMaxDynamicSharedMemorySize, LDS_K1);
  k1_kernel<<<256, 768, LDS_K1, stream>>>(x, wqkv, wo, wg, wu, wd, (char*)d_ws);
  k2_kernel<<<256, 256, 61440, stream>>>(x, (char*)d_ws, (__hip_bfloat16*)d_out);
}
```

```cpp
#include <hip/hip_runtime.h>
#include <hip/hip_bf16.h>
#include <type_traits>

typedef short s16x4 __attribute__((ext_vector_type(4)));
typedef short s16x8 __attribute__((ext_vector_type(8)));
typedef __bf16 bf16x2 __attribute__((ext_vector_type(2)));
typedef __bf16 bf16x8 __attribute__((ext_vector_type(8)));
typedef float f32x2 __attribute__((ext_vector_type(2)));
typedef float f32x4 __attribute__((ext_vector_type(4)));
typedef unsigned u32x2 __attribute__((ext_vector_type(2)));
typedef unsigned u32x4 __attribute__((ext_vector_type(4)));
#define LDSP(T, a) ((__attribute__((address_space(3))) T*)(size_t)(unsigned)(a))

constexpr int S_ = 2048, D_ = 640, F_ = 1536;
constexpr int NKS2 = 68;
constexpr size_t WS_A2 = 0;
constexpr size_t WS_W2 = 9437184;

constexpr int A_SLOT = 22528;
constexpr int LDS_A = 0;
constexpr int B_SLOT = 16384;
constexpr int LDS_B = 3 * A_SLOT;
constexpr int LDS_ST = LDS_B + 5 * B_SLOT;
constexpr int LDS_CS = LDS_ST + 4 * 176 * 8;
constexpr int LDS_DUMMY = LDS_CS + 1024;
constexpr int LDS_K1 = LDS_DUMMY + 1024;
constexpr int QROW = 488;

__device__ __forceinline__ void dma16(const void* sbase, unsigned voff, unsigned lds_addr) {
  unsigned keep;
  asm volatile("s_mov_b32 %0, m0\n\ts_mov_b32 m0, %3\n\ts_nop 0\n\tglobal_load_lds_dwordx4 %1, %2\n\ts_mov_b32 m0, %0"
               : "=&s"(keep) : "v"(voff), "s"(sbase), "s"(lds_addr) : "memory");
}
template <int N> __device__ __forceinline__ void wait_vm() { asm volatile("s_waitcnt vmcnt(%0)" ::"i"(N) : "memory"); }
__device__ __forceinline__ void wait_lgkm0() {
  __builtin_amdgcn_s_waitcnt(0xC07F);
  asm volatile("" ::: "memory");
}

__device__ __forceinline__ bf16x8 tr_frag(unsigned a0, unsigned a1) {
  s16x4 lo = __builtin_amdgcn_ds_read_tr16_b64_v4i16(LDSP(s16x4, a0));
  s16x4 hi = __builtin_amdgcn_ds_read_tr16_b64_v4i16(LDSP(s16x4, a1));
  s16x8 v = __builtin_shufflevector(lo, hi, 0, 1, 2, 3, 4, 5, 6, 7);
  return __builtin_bit_cast(bf16x8, v);
}
__device__ __forceinline__ void mfma_acc(f32x4& c, const bf16x8& a, const bf16x8& b) {
  asm volatile("v_mfma_f32_16x16x32_bf16 %0, %1, %2, %0" : "+a"(c) : "v"(a), "v"(b));
}
__device__ __forceinline__ void mfma_accv(f32x4& c, const bf16x8& a, const bf16x8& b) {
  asm volatile("v_mfma_f32_16x16x32_bf16 %0, %1, %2, %0" : "+v"(c) : "v"(a), "v"(b));
}
__device__ __forceinline__ void st_wt(u32x4* p, u32x4 v) {
  asm volatile("global_store_dwordx4 %0, %1, off sc0 sc1\n\ts_nop 1" :: "v"(p), "v"(v) : "memory");
}
__device__ __forceinline__ void swap16(float& a, float& b) {
  asm volatile("s_nop 1\n\tv_permlane16_swap_b32 %0, %1" : "+v"(a), "+v"(b));
}
__device__ __forceinline__ void swap32(float& a, float& b) {
  asm volatile("s_nop 1\n\tv_permlane32_swap_b32 %0, %1" : "+v"(a), "+v"(b));
}
__device__ __forceinline__ float red_max4(float a) {
  float b = a;
  swap16(a, b); a = fmaxf(a, b); b = a;
  swap32(a, b); return fmaxf(a, b);
}
__device__ __forceinline__ float red_sum4(float a) {
  float b = a;
  swap16(a, b); a = a + b; b = a;
  swap32(a, b); return a + b;
}
__device__ __forceinline__ unsigned pack2(float a, float b) {
  bf16x2 p = {(__bf16)a, (__bf16)b};
  return __builtin_bit_cast(unsigned, p);
}

#define STAMP(i)
#define STAMP_DECL
#define STAMP_OUT(k)
template <int I, int N, class Fn> __device__ __forceinline__ void static_for(Fn&& f) {
  if constexpr (I < N) { f(std::integral_constant<int, I>{}); static_for<I + 1, N>(f); }
}

#define SB() __builtin_amdgcn_sched_barrier(0)
#define PC_BACK 0

__global__ __launch_bounds__(768, 3) void k1_kernel(const __hip_bfloat16* __restrict__ x,
                                                    const __hip_bfloat16* __restrict__ wqkv,
                                                    const __hip_bfloat16* __restrict__ wo,
                                                    const __hip_bfloat16* __restrict__ wg,
                                                    const __hip_bfloat16* __restrict__ wu,
                                                    const __hip_bfloat16* __restrict__ wd,
                                                    char* __restrict__ ws) {
  STAMP_DECL;
  STAMP(0);
  const int tid = threadIdx.x, lane = tid & 63;
  const int wave = __builtin_amdgcn_readfirstlane(tid >> 6);
  const int mh = (wave >> 2) & 1, wq = wave & 3;
  const int q = lane >> 4, c = lane & 15;

  const int xcd = blockIdx.x & 7, idx = blockIdx.x >> 3;
  const int xr = xcd >> 2, xc = xcd & 3;
  const int natt = xr == 0 ? 14 : 12;
  const bool is_att = idx < natt;
  int tok0, cbk = 0, hg = 0, job = 1000;
  if (is_att) {
    const int ab = (xr == 0 ? 0 : 7) + (idx >> 1);
    hg = 2 * xc + (idx & 1);
    tok0 = 160 * ab - 16;
  } else {
    const int j = idx - natt;
    if (j >= 18) return;
    const int rb = 6 * xr + j / 3;
    cbk = 3 * xc + j % 3;
    tok0 = 175 * rb - 1;
    job = rb * 12 + cbk;
  }
  int ntile[4];
#pragma unroll
  for (int n = 0; n < 4; ++n) ntile[n] = is_att ? 4 * wq + n : (n < 2 ? 2 * wq + n : 8 + 2 * wq + n - 2);
  f32x4 acc[6][4];

  if (wave >= 8) {
    const int L = wq;
    unsigned b_voff[4];
    const char* b_base[2];
    int b_step;
#pragma unroll
    for (int j = 0; j < 4; ++j) {
      const int row = 4 * (L + 4 * (j & 1)) + (lane >> 4);
      const int hsw = (row & 3) | (((row >> 3) & 1) << 2);
      const int g = (lane & 15) ^ (hsw << 1);
      if (is_att) {
        int Lc = 16 * (j >> 1) + g;
        Lc = Lc > 29 ? 29 : Lc;
        const int sec = Lc / 10;
        b_voff[j] = (unsigned)(row * 3840 + (640 * sec + 80 * hg + 8 * (Lc - 10 * sec)) * 2);
      } else {
        b_voff[j] = (unsigned)(row * 3072 + (128 * cbk + 8 * g) * 2);
      }
    }
    if (is_att) { b_base[0] = (const char*)wqkv; b_base[1] = (const char*)wqkv; b_step = 32 * 3840; }
    else { b_base[0] = (const char*)wg; b_base[1] = (const char*)wu; b_step = 32 * 3072; }
    auto issueB = [&](int j, unsigned slot_off) {
#pragma unroll
      for (int e = 0; e < 4; ++e)
        dma16(b_base[e >> 1] + (size_t)j * b_step, b_voff[e], LDS_B + slot_off + (L + 4 * e) * 1024);
    };
    issueB(0, 0);
    unsigned a_voff[6];
#pragma unroll
    for (int e = 0; e < 6; ++e) {
      const int ii = (L + 4 * e) < 22 ? L + 4 * e : L + 16;
      const int row = 8 * ii + (lane >> 3);
      int tok = tok0 + row;
      tok = tok < 0 ? 0 : (tok > S_ - 1 ? S_ - 1 : tok);
      a_voff[e] = (unsigned)(tok * 1280 + (((lane & 7) ^ ((row >> 1) & 7)) << 4));
    }
    const unsigned a_dst5 = L < 2 ? 20480u : 16384u;
    const bool a_on0 = L != 0 || lane >= 8;
    auto issueA = [&](int j, unsigned slot_off) {
      const char* ab = (const char*)x + j * 128;
      const unsigned dst = LDS_A + slot_off + L * 1024;
      if (a_on0) dma16(ab, a_voff[0], dst);
#pragma unroll
      for (int e = 1; e < 5; ++e) dma16(ab, a_voff[e], dst + e * 4096);
      dma16(ab, a_voff[5], dst + a_dst5);
    };
    issueA(0, 0); issueB(1, B_SLOT); issueA(1, A_SLOT); issueB(2, 2 * B_SLOT);
    issueB(3, 3 * B_SLOT); issueA(2, 2 * A_SLOT); issueB(4, 4 * B_SLOT);
    if (tid - 512 < 24) {
      u32x4 ones = {0x3F803F80u, 0x3F803F80u, 0x3F803F80u, 0x3F803F80u};
      *LDSP(u32x4, LDS_A + ((tid - 512) >> 3) * A_SLOT + (tid & 7) * 16) = ones;
    }
    f32x4 gq[3], gs[3];
#pragma unroll
    for (int i = 0; i < 3; ++i) { gq[i] = f32x4{0.f, 0.f, 0.f, 0.f}; gs[i] = f32x4{0.f, 0.f, 0.f, 0.f}; }
    const bf16x8 ones8 = __builtin_bit_cast(bf16x8, u32x4{0x3F803F80u, 0x3F803F80u, 0x3F803F80u, 0x3F803F80u});
    const unsigned s_rd = LDS_A + L * 2048 + c * 128;
    auto stats = [&](int sub, unsigned ao) {
      const unsigned ad = s_rd + ao + (((4 * sub + q) ^ ((c >> 1) & 7)) << 4);
      bf16x8 f[3];
#pragma unroll
      for (int i = 0; i < 3; ++i) f[i] = *LDSP(bf16x8, ad + i * 8192);
#pragma unroll
      for (int i = 0; i < 3; ++i) {
        gq[i] = __builtin_amdgcn_mfma_f32_16x16x32_bf16(f[i], f[i], gq[i], 0, 0, 0);
        gs[i] = __builtin_amdgcn_mfma_f32_16x16x32_bf16(ones8, f[i], gs[i], 0, 0, 0);
      }
    };
    wait_vm<28>();
    wait_lgkm0();
    __builtin_amdgcn_s_barrier();
    unsigned bo = 0, ao = 0, aop = 2 * A_SLOT;
#pragma unroll 1
    for (int t = 0; t < 20; t += 2) {
      const unsigned bo1 = bo == 4 * B_SLOT ? 0 : bo + B_SLOT;
      const unsigned bo2 = bo1 == 4 * B_SLOT ? 0 : bo1 + B_SLOT;
      if (t == 0) wait_vm<24>(); else if (t <= 14) wait_vm<18>(); else if (t == 16) wait_vm<14>(); else wait_vm<6>();
      wait_lgkm0();
      __builtin_amdgcn_s_barrier();
      if (t >= 2 && t <= 14) issueA((t >> 1) + 2, aop);
      if (t == 16) {
        const int jb = job < 136 ? job : 0;
        const int ks = jb >> 1, half = jb & 1;
        const char* wb = ks < 20 ? (const char*)wo + (size_t)ks * 32 * 1280 : (const char*)wd + (size_t)(ks - 20) * 32 * 1280;
#pragma unroll
        for (int e = 0; e < 6; ++e) {
          const int pe = e < 5 ? e : 4;
          const int o = 1024 * (L + 4 * pe) + 16 * lane;
          const int row = o / 640;
          dma16(wb, (unsigned)(row * 1280 + 640 * half + (o - row * 640)), LDS_A + A_SLOT + (L + 4 * pe) * 1024);
        }
      }
      if (t <= 14) issueB(t + 5, bo);
      stats(0, ao);
      if (t == 0) wait_vm<18>(); else if (t <= 12) wait_vm<18>(); else if (t == 14) wait_vm<18>(); else if (t == 16) wait_vm<10>(); else wait_vm<0>();
      wait_lgkm0();
      __builtin_amdgcn_s_barrier();
      if (t <= 12) issueB(t + 6, bo1);
      stats(1, ao);
      bo = bo2; aop = ao; ao = ao == 2 * A_SLOT ? 0 : ao + A_SLOT;
    }
#pragma unroll
    for (int i = 0; i < 3; ++i) {
      const int r = c & 3;
      const float b = r == 0 ? gq[i][0] : (r == 1 ? gq[i][1] : (r == 2 ? gq[i][2] : gq[i][3]));
      if (q == (c >> 2) && L + 4 * i < 11) {
        const float mu = gs[i][0] * (1.0f / D_);
        float var = b * (1.0f / D_) - mu * mu;
        var = var < 0.f ? 0.f : var;
        const float rs = __builtin_amdgcn_rsqf(var + 1e-5f);
        *LDSP(f32x2, LDS_ST + ((L + 4 * i) * 16 + c) * 8) = f32x2{rs, -mu * rs};
      }
    }
#pragma unroll
    for (int m = 0; m < 6; ++m)
#pragma unroll
      for (int n = 0; n < 4; ++n) acc[m][n] = f32x4{0.f, 0.f, 0.f, 0.f};
  } else {
    unsigned code_pf = 0;
    if (wave < 6) {
      const char* pc = (const char*)(__builtin_amdgcn_s_getpc() & ~255ull);
      code_pf = *(const unsigned*)(pc - PC_BACK + wave * 4096 + lane * 64);
    }
    unsigned a_rd[2];
#pragma unroll
    for (int s = 0; s < 2; ++s)
      a_rd[s] = LDS_A + mh * 6 * 2048 + c * 128 + (((4 * s + q) ^ ((c >> 1) & 7)) << 4);
    unsigned b_rd[4];
    {
      const int a = c >> 2, p = c & 3;
      const int hh = a | ((q & 1) << 2);
#pragma unroll
      for (int n = 0; n < 4; ++n)
        b_rd[n] = LDS_B + (ntile[n] >> 3) * 8192 + (8 * q + a) * 256 + (((ntile[n] & 7) ^ hh) << 5) + 8 * p;
    }
#pragma unroll
    for (int m = 0; m < 6; ++m)
#pragma unroll
      for (int n = 0; n < 4; ++n) {
        acc[m][n] = f32x4{0.f, 0.f, 0.f, 0.f};
        if (m < 5) asm volatile("" : "+a"(acc[m][n])); else asm volatile("s_nop 1" : "+v"(acc[m][n]));
      }
    bf16x8 af[6], bfr[2][4];
    auto step = [&](auto par, unsigned aon, unsigned bon) {
      constexpr int P = decltype(par)::value;
      __builtin_amdgcn_s_waitcnt(0xC27F);
      asm volatile("" ::: "memory");
      __builtin_amdgcn_s_barrier();
      SB();
      static_for<0, 6>([&](auto mc) {
        constexpr int m = decltype(mc)::value;
        if (m < 5 || mh == 0) {
#pragma unroll
          for (int n = 0; n < 4; ++n) {
            if constexpr (m < 5) mfma_acc(acc[m][n], bfr[P][n], af[m]);
            else mfma_accv(acc[m][n], bfr[P][n], af[m]);
          }
        }
        SB();
        af[m] = *LDSP(bf16x8, a_rd[1 - P] + aon + m * 2048);
        if constexpr (m < 4) bfr[1 - P][m] = tr_frag(b_rd[m] + bon, b_rd[m] + bon + 1024);
        SB();
      });
    };
    using P0 = std::integral_constant<int, 0>;
    using P1 = std::integral_constant<int, 1>;
    __builtin_amdgcn_s_barrier();
#pragma unroll
    for (int n = 0; n < 4; ++n) bfr[0][n] = tr_frag(b_rd[n], b_rd[n] + 1024);
#pragma unroll
    for (int m = 0; m < 6; ++m) af[m] = *LDSP(bf16x8, a_rd[0] + m * 2048);
    STAMP(1);
    unsigned bo = 0, ao = 0;
#pragma unroll 1
    for (int t = 0; t < 20; t += 2) {
      const unsigned bo1 = bo == 4 * B_SLOT ? 0 : bo + B_SLOT;
      const unsigned bo2 = bo1 == 4 * B_SLOT ? 0 : bo1 + B_SLOT;
      const unsigned ao1 = ao == 2 * A_SLOT ? 0 : ao + A_SLOT;
      step(P0{}, ao, bo1);
      step(P1{}, ao1, bo2);
      bo = bo2; ao = ao1;
    }
    asm volatile("s_nop 15\n\ts_nop 15" ::: "memory");
    asm volatile("" :: "v"(code_pf));
    STAMP(6);
    if (mh == 0 && c == 0) {
#pragma unroll
      for (int n = 0; n < 4; ++n) *LDSP(f32x4, LDS_CS + (ntile[n] * 16 + 4 * q) * 4) = acc[0][n];
    }
  }
  wait_lgkm0();
  __builtin_amdgcn_s_barrier();

  f32x4 cs[4];
#pragma unroll
  for (int n = 0; n < 4; ++n) cs[n] = *LDSP(f32x4, LDS_CS + (ntile[n] * 16 + 4 * q) * 4);
  float rstd[6], nmr[6];
#pragma unroll
  for (int m = 0; m < 6; ++m) {
    const f32x2 v = *LDSP(f32x2, LDS_ST + ((6 * mh + m) * 16 + c) * 8);
    rstd[m] = v[0]; nmr[m] = v[1];
  }

  char* a2 = ws + WS_A2;
  STAMP(7);
  if (!is_att) {
    const int ks = 20 + 4 * cbk + wq;
    constexpr float NL2E = -1.4426950408889634f;
    if (wave < 8) {
#pragma unroll
    for (int m = 0; m < 6; ++m) {
      if (m < 5 || mh == 0) {
        const int trow = (6 * mh + m) * 16 + c;
        const int tok = tok0 + trow;
        const float rg = rstd[m] * NL2E, ng = nmr[m] * NL2E;
        float hv[8];
#pragma unroll
        for (int n = 0; n < 2; ++n)
#pragma unroll
          for (int r = 0; r < 4; ++r) {
            const float e = __builtin_amdgcn_exp2f(__builtin_fmaf(acc[m][n][r], rg, ng * cs[n][r]));
            const float u = __builtin_fmaf(acc[m][n + 2][r], rstd[m], nmr[m] * cs[n + 2][r]);
            hv[4 * n + r] = u * __builtin_amdgcn_rcpf(1.0f + e);
          }
        u32x4 pk = {pack2(hv[0], hv[1]), pack2(hv[2], hv[3]), pack2(hv[4], hv[5]), pack2(hv[6], hv[7])};
        if (trow >= 1 && tok < S_)
          st_wt((u32x4*)(a2 + (size_t)(((tok >> 6) * NKS2 + ks) * 4 + ((tok >> 4) & 3)) * 1024 + (16 * q + (tok & 15)) * 16), pk);
      }
    }
    } else if (job < 136) {
      const int ks2 = job >> 1, half = job & 1;
      const int a = c >> 2, p = c & 3;
#pragma unroll 1
      for (int u = wq; u < 20; u += 4) {
        const int cl = u / 5, n5 = u - 5 * cl;
        const int colst = n5 < 4 ? 32 * (n5 >> 1) + 8 * p + 4 * (n5 & 1) : 64 + 4 * p;
        const unsigned ad = LDS_A + 1 * A_SLOT + (4 * q + a) * 640 + (80 * cl + colst) * 2;
        bf16x8 f = tr_frag(ad, ad + 16 * 640);
        const int cb2 = 4 * half + cl;
        st_wt((u32x4*)(ws + WS_W2 + (size_t)((cb2 * NKS2 + ks2) * 5 + n5) * 1024 + lane * 16), __builtin_bit_cast(u32x4, f));
      }
    }
  } else {
#pragma unroll
    for (int m = 0; m < 6; ++m) {
      if (wave < 8 && (m < 5 || mh == 0)) {
        const int trow = (6 * mh + m) * 16 + c;
#pragma unroll
        for (int n = 0; n < 4; ++n) {
          float y[4];
#pragma unroll
          for (int r = 0; r < 4; ++r) y[r] = __builtin_fmaf(acc[m][n][r], rstd[m], nmr[m] * cs[n][r]);
          if (n < 3 || wq < 3)
            *LDSP(u32x2, trow * QROW + (ntile[n] * 16 + 4 * q) * 2) = u32x2{pack2(y[0], y[1]), pack2(y[2], y[3])};
        }
      }
    }
    float bias[8];
#pragma unroll
    for (int j = 0; j < 8; ++j) {
      const int diff = 16 + c - (16 * (j >> 2) + 4 * q + (j & 3));
      bias[j] = (diff >= 0 && diff <= 14 && (diff & 1) == 0) ? 0.f : -3.0e38f;
    }
    const float sl2 = 0.22360679774997896f * 1.4426950408889634f;
    const int dofs = q < 2 ? 16 * q : 32;
    const unsigned qk_l = c * QROW + dofs;
    const unsigned v_l = (4 * q + (c >> 2)) * QROW + 320 + 8 * (c & 3);
    wait_lgkm0();
    __builtin_amdgcn_s_barrier();
    STAMP(8);
#pragma unroll 1
    for (int unit = wave; unit < 40; unit += 12) {
      const int mq = 1 + (unit >> 2), hh = unit & 3;
      const unsigned ub = 16 * (mq - 1) * QROW + 40 * hh;
      u32x2 q0 = *LDSP(u32x2, ub + qk_l + 16 * QROW), q1 = *LDSP(u32x2, ub + qk_l + 16 * QROW + 8);
      u32x2 k00 = *LDSP(u32x2, ub + qk_l + 160), k01 = *LDSP(u32x2, ub + qk_l + 168);
      u32x2 k10 = *LDSP(u32x2, ub + qk_l + 16 * QROW + 160), k11 = *LDSP(u32x2, ub + qk_l + 16 * QROW + 168);
      const bf16x8 vf0 = tr_frag(ub + v_l, ub + v_l + 16 * QROW);
      const bf16x8 vf1 = tr_frag(ub + v_l + 32, ub + v_l + 32 + 16 * QROW);
      if (q >= 2) { q1 = u32x2{0u, 0u}; }
      if (q == 3) { q0 = u32x2{0u, 0u}; }
      const bf16x8 qf = __builtin_bit_cast(bf16x8, u32x4{q0[0], q0[1], q1[0], q1[1]});
      const f32x4 z4 = {0.f, 0.f, 0.f, 0.f};
      const f32x4 s0 = __builtin_amdgcn_mfma_f32_16x16x32_bf16(__builtin_bit_cast(bf16x8, u32x4{k00[0], k00[1], k01[0], k01[1]}), qf, z4, 0, 0, 0);
      const f32x4 s1 = __builtin_amdgcn_mfma_f32_16x16x32_bf16(__builtin_bit_cast(bf16x8, u32x4{k10[0], k10[1], k11[0], k11[1]}), qf, z4, 0, 0, 0);
      float lg[8];
#pragma unroll
      for (int j = 0; j < 4; ++j) {
        lg[j] = __builtin_fmaf(s0[j], sl2, bias[j]);
        lg[4 + j] = __builtin_fmaf(s1[j], sl2, bias[4 + j]);
      }
      if (tok0 + 16 * (mq - 1) < 0) {
#pragma unroll
        for (int j = 0; j < 4; ++j) lg[j] = -3.0e38f;
      }
      float mx = fmaxf(fmaxf(fmaxf(lg[0], lg[1]), fmaxf(lg[2], lg[3])), fmaxf(fmaxf(lg[4], lg[5]), fmaxf(lg[6], lg[7])));
      mx = red_max4(mx);
      float sum = 0.f;
#pragma unroll
      for (int j = 0; j < 8; ++j) { lg[j] = __builtin_amdgcn_exp2f(lg[j] - mx); sum += lg[j]; }
      const float inv = __builtin_amdgcn_rcpf(red_sum4(sum));
      const bf16x8 pf = __builtin_bit_cast(bf16x8, u32x4{pack2(lg[0] * inv, lg[1] * inv), pack2(lg[2] * inv, lg[3] * inv),
                                                         pack2(lg[4] * inv, lg[5] * inv), pack2(lg[6] * inv, lg[7] * inv)});
      const f32x4 o0 = __builtin_amdgcn_mfma_f32_16x16x32_bf16(vf0, pf, z4, 0, 0, 0);
      const f32x4 o1 = __builtin_amdgcn_mfma_f32_16x16x32_bf16(vf1, pf, z4, 0, 0, 0);
      const int tokb = tok0 + 16 * mq;
      if (tokb < S_) {
        char* ob = a2 + (size_t)((tokb >> 6) * NKS2 * 4 + ((tokb >> 4) & 3)) * 1024 + c * 16;
        const int col0 = 80 * hg + 20 * hh + 4 * q;
        *(u32x2*)(ob + (col0 >> 5) * 4096 + ((col0 >> 2) & 3) * 256 + ((col0 >> 4) & 1) * 8) = u32x2{pack2(o0[0], o0[1]), pack2(o0[2], o0[3])};
        const int col1 = 80 * hg + 20 * hh + 16;
        if (q == 0)
          *(u32x2*)(ob + (col1 >> 5) * 4096 + ((col1 >> 2) & 3) * 256 + ((col1 >> 4) & 1) * 8) = u32x2{pack2(o1[0], o1[1]), pack2(o1[2], o1[3])};
      }
    }
  }
  STAMP(9);
  STAMP(10);
  STAMP_OUT(0);
}

constexpr int K2_DEPTH = 6;
__global__ __launch_bounds__(256, 1) void k2_kernel(const __hip_bfloat16* __restrict__ x,
                                                    char* __restrict__ ws,
                                                    __hip_bfloat16* __restrict__ out) {
  const int tid = threadIdx.x, lane = tid & 63;
  const int wave = __builtin_amdgcn_readfirstlane(tid >> 6);
  const int q = lane >> 4, c = lane & 15;
  const int xcd = blockIdx.x & 7, idx = blockIdx.x >> 3;
  const int rb = (xcd >> 1) * 8 + (idx >> 2), cb = (xcd & 1) * 4 + (idx & 3);
  STAMP_DECL;
  STAMP(0);

  const char* ap = ws + WS_A2 + (size_t)(rb * NKS2 + 17 * wave) * 4096 + lane * 16;
  const char* wp = ws + WS_W2 + (size_t)(cb * NKS2 + 17 * wave) * 5120 + lane * 16;

  bf16x8 fa[K2_DEPTH][4], fw[K2_DEPTH][5];
  f32x4 acc[4][5];
#pragma unroll
  for (int m = 0; m < 4; ++m)
#pragma unroll
    for (int n = 0; n < 5; ++n) acc[m][n] = f32x4{0.f, 0.f, 0.f, 0.f};

  auto ld = [&](int i) {
    const int s = i % K2_DEPTH;
#pragma unroll
    for (int m = 0; m < 4; ++m) fa[s][m] = *(const bf16x8*)(ap + i * 4096 + m * 1024);
#pragma unroll
    for (int n = 0; n < 5; ++n) fw[s][n] = *(const bf16x8*)(wp + i * 5120 + n * 1024);
  };
#pragma unroll
  for (int i = 0; i < K2_DEPTH; ++i) ld(i);
  __builtin_amdgcn_sched_barrier(0);
#pragma unroll
  for (int i = 0; i < 17; ++i) {
    const int s = i % K2_DEPTH;
#pragma unroll
    for (int m = 0; m < 4; ++m)
#pragma unroll
      for (int n = 0; n < 5; ++n)
        acc[m][n] = __builtin_amdgcn_mfma_f32_16x16x32_bf16(fw[s][n], fa[s][m], acc[m][n], 0, 0, 0);
    __builtin_amdgcn_sched_barrier(0);
    if (i + K2_DEPTH < 17) ld(i + K2_DEPTH);
    __builtin_amdgcn_sched_barrier(0);
  }

  const int tok = rb * 64 + wave * 16 + c;
  const size_t obase = (size_t)tok * D_ + cb * 80;
  const u32x4 x0 = *(const u32x4*)(x + obase + 8 * q);
  const u32x4 x1 = *(const u32x4*)(x + obase + 32 + 8 * q);
  const u32x2 x2 = *(const u32x2*)(x + obase + 64 + 4 * q);

#pragma unroll
  for (int m = 0; m < 4; ++m) {
    if (m != wave) {
      const int si = wave < m ? wave : wave - 1;
#pragma unroll
      for (int n = 0; n < 5; ++n) *LDSP(f32x4, ((m * 3 + si) * 5 + n) * 1024 + lane * 16) = acc[m][n];
    }
  }
  __syncthreads();
  f32x4 r[5];
#pragma unroll
  for (int n = 0; n < 5; ++n) {
    r[n] = wave == 0 ? acc[0][n] : (wave == 1 ? acc[1][n] : (wave == 2 ? acc[2][n] : acc[3][n]));
#pragma unroll
    for (int si = 0; si < 3; ++si) r[n] += *LDSP(f32x4, ((wave * 3 + si) * 5 + n) * 1024 + lane * 16);
  }
  auto lo = [](unsigned v) { return __builtin_bit_cast(float, v << 16); };
  auto hi = [](unsigned v) { return __builtin_bit_cast(float, v & 0xFFFF0000u); };
  u32x4 o0 = {pack2(r[0][0] + lo(x0[0]), r[0][1] + hi(x0[0])), pack2(r[0][2] + lo(x0[1]), r[0][3] + hi(x0[1])),
              pack2(r[1][0] + lo(x0[2]), r[1][1] + hi(x0[2])), pack2(r[1][2] + lo(x0[3]), r[1][3] + hi(x0[3]))};
  u32x4 o1 = {pack2(r[2][0] + lo(x1[0]), r[2][1] + hi(x1[0])), pack2(r[2][2] + lo(x1[1]), r[2][3] + hi(x1[1])),
              pack2(r[3][0] + lo(x1[2]), r[3][1] + hi(x1[2])), pack2(r[3][2] + lo(x1[3]), r[3][3] + hi(x1[3]))};
  u32x2 o2 = {pack2(r[4][0] + lo(x2[0]), r[4][1] + hi(x2[0])), pack2(r[4][2] + lo(x2[1]), r[4][3] + hi(x2[1]))};
  *(u32x4*)(out + obase + 8 * q) = o0;
  *(u32x4*)(out + obase + 32 + 8 * q) = o1;
  *(u32x2*)(out + obase + 64 + 4 * q) = o2;
  STAMP(6);
  STAMP_OUT(1);
}

extern "C" void kernel_launch(void* const* d_in, const int* in_sizes, int n_in,
                              void* d_out, int out_size, void* d_ws, size_t ws_size,
                              hipStream_t stream) {
  const __hip_bfloat16* x = (const __hip_bfloat16*)d_in[0];
  const __hip_bfloat16* wqkv = (const __hip_bfloat16*)d_in[1];
  const __hip_bfloat16* wo = (const __hip_bfloat16*)d_in[2];
  const __hip_bfloat16* wg = (const __hip_bfloat16*)d_in[4];
  const __hip_bfloat16* wu = (const __hip_bfloat16*)d_in[5];
  const __hip_bfloat16* wd = (const __hip_bfloat16*)d_in[6];
  (void)hipFuncSetAttribute((const void*)k1_kernel, hipFuncAttributeMaxDynamicSharedMemorySize, LDS_K1);
  k1_kernel<<<256, 768, LDS_K1, stream>>>(x, wqkv, wo, wg, wu, wd, (char*)d_ws);
  k2_kernel<<<256, 256, 61440, stream>>>(x, (char*)d_ws, (__hip_bfloat16*)d_out);
}
```

```cpp
#include <hip/hip_runtime.h>
#include <hip/hip_bf16.h>
#include <type_traits>

typedef short s16x4 __attribute__((ext_vector_type(4)));
typedef short s16x8 __attribute__((ext_vector_type(8)));
typedef __bf16 bf16x2 __attribute__((ext_vector_type(2)));
typedef __bf16 bf16x8 __attribute__((ext_vector_type(8)));
typedef float f32x2 __attribute__((ext_vector_type(2)));
typedef float f32x4 __attribute__((ext_vector_type(4)));
typedef unsigned u32x2 __attribute__((ext_vector_type(2)));
typedef unsigned u32x4 __attribute__((ext_vector_type(4)));
#define LDSP(T, a) ((__attribute__((address_space(3))) T*)(size_t)(unsigned)(a))

constexpr int S_ = 2048, D_ = 640, F_ = 1536;
constexpr int NKS2 = 68;
constexpr size_t WS_A2 = 0;
constexpr size_t WS_W2 = 9437184;

constexpr int A_SLOT = 24576;
constexpr int LDS_A = 0;
constexpr int B_SLOT = 16384;
constexpr int LDS_B = 3 * A_SLOT;
constexpr int LDS_ST = LDS_B + 5 * B_SLOT;
constexpr int LDS_CS = LDS_ST + 192 * 8;
constexpr int LDS_K1 = LDS_CS + 1024;
constexpr int QROW = 488;

__device__ __forceinline__ void dma16(const void* sbase, unsigned voff, unsigned lds_addr) {
  unsigned keep;
  asm volatile("s_mov_b32 %0, m0\n\ts_mov_b32 m0, %3\n\ts_nop 0\n\tglobal_load_lds_dwordx4 %1, %2\n\ts_mov_b32 m0, %0"
               : "=&s"(keep) : "v"(voff), "s"(sbase), "s"(lds_addr) : "memory");
}
template <int N> __device__ __forceinline__ void wait_vm() { asm volatile("s_waitcnt vmcnt(%0)" ::"i"(N) : "memory"); }
__device__ __forceinline__ void wait_lgkm0() {
  __builtin_amdgcn_s_waitcnt(0xC07F);
  asm volatile("" ::: "memory");
}

__device__ __forceinline__ bf16x8 tr_frag(unsigned a0, unsigned a1) {
  s16x4 lo = __builtin_amdgcn_ds_read_tr16_b64_v4i16(LDSP(s16x4, a0));
  s16x4 hi = __builtin_amdgcn_ds_read_tr16_b64_v4i16(LDSP(s16x4, a1));
  s16x8 v = __builtin_shufflevector(lo, hi, 0, 1, 2, 3, 4, 5, 6, 7);
  return __builtin_bit_cast(bf16x8, v);
}
__device__ __forceinline__ void mfma_acc(f32x4& c, const bf16x8& a, const bf16x8& b) {
  asm volatile("v_mfma_f32_16x16x32_bf16 %0, %1, %2, %0" : "+a"(c) : "v"(a), "v"(b));
}
__device__ __forceinline__ void mfma_accv(f32x4& c, const bf16x8& a, const bf16x8& b) {
  asm volatile("v_mfma_f32_16x16x32_bf16 %0, %1, %2, %0" : "+v"(c) : "v"(a), "v"(b));
}
__device__ __forceinline__ void st_wt(u32x4* p, u32x4 v) {
  asm volatile("global_store_dwordx4 %0, %1, off sc0 sc1\n\ts_nop 1" :: "v"(p), "v"(v) : "memory");
}
__device__ __forceinline__ void swap16(float& a, float& b) {
  asm volatile("s_nop 1\n\tv_permlane16_swap_b32 %0, %1" : "+v"(a), "+v"(b));
}
__device__ __forceinline__ void swap32(float& a, float& b) {
  asm volatile("s_nop 1\n\tv_permlane32_swap_b32 %0, %1" : "+v"(a), "+v"(b));
}
__device__ __forceinline__ float red_max4(float a) {
  float b = a;
  swap16(a, b); a = fmaxf(a, b); b = a;
  swap32(a, b); return fmaxf(a, b);
}
__device__ __forceinline__ float red_sum4(float a) {
  float b = a;
  swap16(a, b); a = a + b; b = a;
  swap32(a, b); return a + b;
}
__device__ __forceinline__ unsigned pack2(float a, float b) {
  bf16x2 p = {(__bf16)a, (__bf16)b};
  return __builtin_bit_cast(unsigned, p);
}

#define STAMP(i)
#define STAMP_DECL
#define STAMP_OUT(k)
template <int I, int N, class Fn> __device__ __forceinline__ void static_for(Fn&& f) {
  if constexpr (I < N) { f(std::integral_constant<int, I>{}); static_for<I + 1, N>(f); }
}

#define SB() __builtin_amdgcn_sched_barrier(0)
#define PC_BACK 0

__device__ __forceinline__ void retile_w2(char* ws, int job, int u0, int ustep, int lane) {
  const int q = lane >> 4, c = lane & 15;
  const int ks2 = job >> 1, half = job & 1;
  const int a = c >> 2, p = c & 3;
#pragma unroll 1
  for (int u = u0; u < 20; u += ustep) {
    const int cl = u / 5, n5 = u - 5 * cl;
    const int colst = n5 < 4 ? 32 * (n5 >> 1) + 8 * p + 4 * (n5 & 1) : 64 + 4 * p;
    const unsigned ad = LDS_A + 1 * A_SLOT + (4 * q + a) * 640 + (80 * cl + colst) * 2;
    bf16x8 f = tr_frag(ad, ad + 16 * 640);
    const int cb2 = 4 * half + cl;
    st_wt((u32x4*)(ws + WS_W2 + (size_t)((cb2 * NKS2 + ks2) * 5 + n5) * 1024 + lane * 16), __builtin_bit_cast(u32x4, f));
  }
}

__global__ __launch_bounds__(768, 3) void k1_kernel(const __hip_bfloat16* __restrict__ x,
                                                    const __hip_bfloat16* __restrict__ wqkv,
                                                    const __hip_bfloat16* __restrict__ wo,
                                                    const __hip_bfloat16* __restrict__ wg,
                                                    const __hip_bfloat16* __restrict__ wu,
                                                    const __hip_bfloat16* __restrict__ wd,
                                                    char* __restrict__ ws) {
  STAMP_DECL;
  STAMP(0);
  const int tid = threadIdx.x, lane = tid & 63;
  const int wave = __builtin_amdgcn_readfirstlane(tid >> 6);
  const int mh = (wave >> 2) & 1, wq = wave & 3;
  const int q = lane >> 4, c = lane & 15;

  const int xcd = blockIdx.x & 7, idx = blockIdx.x >> 3;
  const int xr = xcd >> 2, xc = xcd & 3;
  const int natt = xr == 0 ? 14 : 16;
  const bool is_att = idx < natt;
  int tok0, cbk = 0, hg = 0, job = 0;
  if (is_att) {
    const int ab = (xr == 0 ? 0 : 7) + (idx >> 1);
    hg = 2 * xc + (idx & 1);
    tok0 = 144 * ab - 16;
  } else {
    const int j = idx - natt;
    if (xr == 1 && j >= 15) {
      const int jb = 132 + xc;
      const char* wb = (const char*)wd + (size_t)((jb >> 1) - 20) * 32 * 1280 + 640 * (jb & 1);
      for (int pc = wave; pc < 20; pc += 12) {
        const int o = 1024 * pc + 16 * lane;
        const int row = o / 640;
        dma16(wb, (unsigned)(row * 1280 + (o - row * 640)), LDS_A + A_SLOT + pc * 1024);
      }
      wait_vm<0>();
      __builtin_amdgcn_s_barrier();
      retile_w2(ws, jb, wave, 12, lane);
      return;
    }
    const int rb = 6 * xr + j / 3;
    cbk = 3 * xc + j % 3;
    tok0 = 191 * rb - 1;
    job = rb * 12 + cbk;
  }
  const int mbase = is_att ? 5 * mh : 6 * mh;
  int ntile[4];
#pragma unroll
  for (int n = 0; n < 4; ++n) ntile[n] = is_att ? 4 * wq + n : (n < 2 ? 2 * wq + n : 8 + 2 * wq + n - 2);
  f32x4 acc[6][4];

  if (wave >= 8) {
    const int L = wq;
    unsigned b_voff[4];
    const char* b_base[2];
    int b_step;
#pragma unroll
    for (int j = 0; j < 4; ++j) {
      const int row = 4 * (L + 4 * (j & 1)) + (lane >> 4);
      const int hsw = (row & 3) | (((row >> 3) & 1) << 2);
      const int g = (lane & 15) ^ (hsw << 1);
      if (is_att) {
        int Lc = 16 * (j >> 1) + g;
        Lc = Lc > 29 ? 29 : Lc;
        const int sec = Lc / 10;
        b_voff[j] = (unsigned)(row * 3840 + (640 * sec + 80 * hg + 8 * (Lc - 10 * sec)) * 2);
      } else {
        b_voff[j] = (unsigned)(row * 3072 + (128 * cbk + 8 * g) * 2);
      }
    }
    if (is_att) { b_base[0] = (const char*)wqkv; b_base[1] = (const char*)wqkv; b_step = 32 * 3840; }
    else { b_base[0] = (const char*)wg; b_base[1] = (const char*)wu; b_step = 32 * 3072; }
    auto issueB = [&](int j, unsigned slot_off) {
#pragma unroll
      for (int e = 0; e < 4; ++e)
        dma16(b_base[e >> 1] + (size_t)j * b_step, b_voff[e], LDS_B + slot_off + (L + 4 * e) * 1024);
    };
    issueB(0, 0);
    unsigned a_voff[6];
#pragma unroll
    for (int e = 0; e < 6; ++e) {
      const int ii = (e < 5 || !is_att) ? L + 4 * e : L + 16;
      const int row = 8 * ii + (lane >> 3);
      int tok = tok0 + row;
      tok = tok < 0 ? 0 : (tok > S_ - 1 ? S_ - 1 : tok);
      a_voff[e] = (unsigned)(tok * 1280 + (((lane & 7) ^ ((row >> 1) & 7)) << 4));
    }
    const unsigned a_dst5 = !is_att ? 20480u : 16384u;
    const bool a_on0 = L != 0 || lane >= 8;
    auto issueA = [&](int j, unsigned slot_off) {
      const char* ab = (const char*)x + j * 128;
      const unsigned dst = LDS_A + slot_off + L * 1024;
      if (a_on0) dma16(ab, a_voff[0], dst);
#pragma unroll
      for (int e = 1; e < 5; ++e) dma16(ab, a_voff[e], dst + e * 4096);
      dma16(ab, a_voff[5], dst + a_dst5);
    };
    issueA(0, 0); issueB(1, B_SLOT); issueA(1, A_SLOT); issueB(2, 2 * B_SLOT);
    issueB(3, 3 * B_SLOT); issueA(2, 2 * A_SLOT); issueB(4, 4 * B_SLOT);
    if (tid - 512 < 24) {
      u32x4 ones = {0x3F803F80u, 0x3F803F80u, 0x3F803F80u, 0x3F803F80u};
      *LDSP(u32x4, LDS_A + ((tid - 512) >> 3) * A_SLOT + (tid & 7) * 16) = ones;
    }
    f32x4 gq[3], gs[3];
#pragma unroll
    for (int i = 0; i < 3; ++i) { gq[i] = f32x4{0.f, 0.f, 0.f, 0.f}; gs[i] = f32x4{0.f, 0.f, 0.f, 0.f}; }
    const bf16x8 ones8 = __builtin_bit_cast(bf16x8, u32x4{0x3F803F80u, 0x3F803F80u, 0x3F803F80u, 0x3F803F80u});
    const unsigned s_rd = LDS_A + L * 2048 + c * 128;
    auto stats = [&](int sub, unsigned ao) {
      const unsigned ad = s_rd + ao + (((4 * sub + q) ^ ((c >> 1) & 7)) << 4);
      bf16x8 f[3];
#pragma unroll
      for (int i = 0; i < 3; ++i) f[i] = *LDSP(bf16x8, ad + i * 8192);
#pragma unroll
      for (int i = 0; i < 3; ++i) {
        gq[i] = __builtin_amdgcn_mfma_f32_16x16x32_bf16(f[i], f[i], gq[i], 0, 0, 0);
        gs[i] = __builtin_amdgcn_mfma_f32_16x16x32_bf16(ones8, f[i], gs[i], 0, 0, 0);
      }
    };
    wait_vm<28>();
    wait_lgkm0();
    __builtin_amdgcn_s_barrier();
    unsigned bo = 0, ao = 0, aop = 2 * A_SLOT;
#pragma unroll 1
    for (int t = 0; t < 20; t += 2) {
      const unsigned bo1 = bo == 4 * B_SLOT ? 0 : bo + B_SLOT;
      const unsigned bo2 = bo1 == 4 * B_SLOT ? 0 : bo1 + B_SLOT;
      if (t == 0) wait_vm<24>(); else if (t <= 14) wait_vm<18>(); else if (t == 16) wait_vm<14>(); else wait_vm<6>();
      wait_lgkm0();
      __builtin_amdgcn_s_barrier();
      if (t >= 2 && t <= 14) issueA((t >> 1) + 2, aop);
      if (t == 16) {
        const int ks = job >> 1, half = job & 1;
        const char* wb = ks < 20 ? (const char*)wo + (size_t)ks * 32 * 1280 : (const char*)wd + (size_t)(ks - 20) * 32 * 1280;
#pragma unroll
        for (int e = 0; e < 6; ++e) {
          const int pe = e < 5 ? e : 4;
          const int o = 1024 * (L + 4 * pe) + 16 * lane;
          const int row = o / 640;
          dma16(wb, (unsigned)(row * 1280 + 640 * half + (o - row * 640)), LDS_A + A_SLOT + (L + 4 * pe) * 1024);
        }
      }
      if (t <= 14) issueB(t + 5, bo);
      stats(0, ao);
      if (t == 0) wait_vm<18>(); else if (t <= 12) wait_vm<18>(); else if (t == 14) wait_vm<18>(); else if (t == 16) wait_vm<10>(); else wait_vm<0>();
      wait_lgkm0();
      __builtin_amdgcn_s_barrier();
      if (t <= 12) issueB(t + 6, bo1);
      stats(1, ao);
      bo = bo2; aop = ao; ao = ao == 2 * A_SLOT ? 0 : ao + A_SLOT;
    }
#pragma unroll
    for (int i = 0; i < 3; ++i) {
      const int r = c & 3;
      const float b = r == 0 ? gq[i][0] : (r == 1 ? gq[i][1] : (r == 2 ? gq[i][2] : gq[i][3]));
      if (q == (c >> 2)) {
        const float mu = gs[i][0] * (1.0f / D_);
        float var = b * (1.0f / D_) - mu * mu;
        var = var < 0.f ? 0.f : var;
        const float rs = __builtin_amdgcn_rsqf(var + 1e-5f);
        *LDSP(f32x2, LDS_ST + ((L + 4 * i) * 16 + c) * 8) = f32x2{rs, -mu * rs};
      }
    }
#pragma unroll
    for (int m = 0; m < 6; ++m)
#pragma unroll
      for (int n = 0; n < 4; ++n) acc[m][n] = f32x4{0.f, 0.f, 0.f, 0.f};
  } else {
    unsigned code_pf = 0;
    if (wave < 6) {
      const char* pc = (const char*)(__builtin_amdgcn_s_getpc() & ~255ull);
      code_pf = *(const unsigned*)(pc - PC_BACK + wave * 4096 + lane * 64);
    }
    unsigned a_rd[2];
#pragma unroll
    for (int s = 0; s < 2; ++s)
      a_rd[s] = LDS_A + mbase * 2048 + c * 128 + (((4 * s + q) ^ ((c >> 1) & 7)) << 4);
    unsigned b_rd[4];
    {
      const int a = c >> 2, p = c & 3;
      const int hh = a | ((q & 1) << 2);
#pragma unroll
      for (int n = 0; n < 4; ++n)
        b_rd[n] = LDS_B + (ntile[n] >> 3) * 8192 + (8 * q + a) * 256 + (((ntile[n] & 7) ^ hh) << 5) + 8 * p;
    }
#pragma unroll
    for (int m = 0; m < 6; ++m)
#pragma unroll
      for (int n = 0; n < 4; ++n) {
        acc[m][n] = f32x4{0.f, 0.f, 0.f, 0.f};
        if (m < 5) asm volatile("" : "+a"(acc[m][n])); else asm volatile("s_nop 1" : "+v"(acc[m][n]));
      }
    bf16x8 af[6], bfr[2][4];
    auto step = [&](auto par, unsigned aon, unsigned bon) {
      constexpr int P = decltype(par)::value;
      __builtin_amdgcn_s_waitcnt(0xC27F);
      asm volatile("" ::: "memory");
      __builtin_amdgcn_s_barrier();
      SB();
      static_for<0, 6>([&](auto mc) {
        constexpr int m = decltype(mc)::value;
        if (m < 5 || !is_att) {
#pragma unroll
          for (int n = 0; n < 4; ++n) {
            if constexpr (m < 5) mfma_acc(acc[m][n], bfr[P][n], af[m]);
            else mfma_accv(acc[m][n], bfr[P][n], af[m]);
          }
        }
        SB();
        af[m] = *LDSP(bf16x8, a_rd[1 - P] + aon + m * 2048);
        if constexpr (m < 4) bfr[1 - P][m] = tr_frag(b_rd[m] + bon, b_rd[m] + bon + 1024);
        SB();
      });
    };
    using P0 = std::integral_constant<int, 0>;
    using P1 = std::integral_constant<int, 1>;
    __builtin_amdgcn_s_barrier();
#pragma unroll
    for (int n = 0; n < 4; ++n) bfr[0][n] = tr_frag(b_rd[n], b_rd[n] + 1024);
#pragma unroll
    for (int m = 0; m < 6; ++m) af[m] = *LDSP(bf16x8, a_rd[0] + m * 2048);
    STAMP(1);
    unsigned bo = 0, ao = 0;
#pragma unroll 1
    for (int t = 0; t < 20; t += 2) {
      const unsigned bo1 = bo == 4 * B_SLOT ? 0 : bo + B_SLOT;
      const unsigned bo2 = bo1 == 4 * B_SLOT ? 0 : bo1 + B_SLOT;
      const unsigned ao1 = ao == 2 * A_SLOT ? 0 : ao + A_SLOT;
      step(P0{}, ao, bo1);
      step(P1{}, ao1, bo2);
      bo = bo2; ao = ao1;
    }
    asm volatile("s_nop 15\n\ts_nop 15" ::: "memory");
    asm volatile("" :: "v"(code_pf));
    STAMP(6);
    if (mh == 0 && c == 0) {
#pragma unroll
      for (int n = 0; n < 4; ++n) *LDSP(f32x4, LDS_CS + (ntile[n] * 16 + 4 * q) * 4) = acc[0][n];
    }
  }
  wait_lgkm0();
  __builtin_amdgcn_s_barrier();

  f32x4 cs[4];
#pragma unroll
  for (int n = 0; n < 4; ++n) cs[n] = *LDSP(f32x4, LDS_CS + (ntile[n] * 16 + 4 * q) * 4);
  float rstd[6], nmr[6];
#pragma unroll
  for (int m = 0; m < 6; ++m) {
    const f32x2 v = *LDSP(f32x2, LDS_ST + ((mbase + m) * 16 + c) * 8);
    rstd[m] = v[0]; nmr[m] = v[1];
  }

  char* a2 = ws + WS_A2;
  STAMP(7);
  if (!is_att) {
    const int ks = 20 + 4 * cbk + wq;
    constexpr float NL2E = -1.4426950408889634f;
    if (wave < 8) {
#pragma unroll
      for (int m = 0; m < 6; ++m) {
        const int trow = (mbase + m) * 16 + c;
        const int tok = tok0 + trow;
        const float rg = rstd[m] * NL2E, ng = nmr[m] * NL2E;
        const f32x2 rg2 = {rg, rg}, ng2 = {ng, ng}, rs2 = {rstd[m], rstd[m]}, nm2 = {nmr[m], nmr[m]};
        unsigned pkv[4];
#pragma unroll
        for (int n = 0; n < 2; ++n)
#pragma unroll
          for (int h = 0; h < 2; ++h) {
            const f32x2 ag = {acc[m][n][2 * h], acc[m][n][2 * h + 1]}, au = {acc[m][n + 2][2 * h], acc[m][n + 2][2 * h + 1]};
            const f32x2 cg = {cs[n][2 * h], cs[n][2 * h + 1]}, cu = {cs[n + 2][2 * h], cs[n + 2][2 * h + 1]};
            const f32x2 eg = __builtin_elementwise_fma(ag, rg2, ng2 * cg);
            const f32x2 uu = __builtin_elementwise_fma(au, rs2, nm2 * cu);
            f32x2 d = {__builtin_amdgcn_exp2f(eg[0]), __builtin_amdgcn_exp2f(eg[1])};
            d = d + f32x2{1.0f, 1.0f};
            const f32x2 hv = uu * f32x2{__builtin_amdgcn_rcpf(d[0]), __builtin_amdgcn_rcpf(d[1])};
            pkv[2 * n + h] = pack2(hv[0], hv[1]);
          }
        if (trow >= 1 && tok < S_)
          st_wt((u32x4*)(a2 + (size_t)(((tok >> 6) * NKS2 + ks) * 4 + ((tok >> 4) & 3)) * 1024 + (16 * q + (tok & 15)) * 16),
                u32x4{pkv[0], pkv[1], pkv[2], pkv[3]});
      }
    } else {
      retile_w2(ws, job, wq, 4, lane);
    }
  } else {
    if (wave < 8) {
#pragma unroll
      for (int m = 0; m < 5; ++m) {
        const int trow = (mbase + m) * 16 + c;
        const f32x2 rs2 = {rstd[m], rstd[m]}, nm2 = {nmr[m], nmr[m]};
#pragma unroll
        for (int n = 0; n < 4; ++n) {
          const f32x2 y0 = __builtin_elementwise_fma(f32x2{acc[m][n][0], acc[m][n][1]}, rs2, nm2 * f32x2{cs[n][0], cs[n][1]});
          const f32x2 y1 = __builtin_elementwise_fma(f32x2{acc[m][n][2], acc[m][n][3]}, rs2, nm2 * f32x2{cs[n][2], cs[n][3]});
          if (n < 3 || wq < 3)
            *LDSP(u32x2, trow * QROW + (ntile[n] * 16 + 4 * q) * 2) = u32x2{pack2(y0[0], y0[1]), pack2(y1[0], y1[1])};
        }
      }
    }
    float bias[8];
#pragma unroll
    for (int j = 0; j < 8; ++j) {
      const int diff = 16 + c - (16 * (j >> 2) + 4 * q + (j & 3));
      bias[j] = (diff >= 0 && diff <= 14 && (diff & 1) == 0) ? 0.f : -3.0e38f;
    }
    const float sl2 = 0.22360679774997896f * 1.4426950408889634f;
    const int dofs = q < 2 ? 16 * q : 32;
    const unsigned qk_l = c * QROW + dofs;
    const unsigned v_l = (4 * q + (c >> 2)) * QROW + 320 + 8 * (c & 3);
    wait_lgkm0();
    __builtin_amdgcn_s_barrier();
    STAMP(8);
#pragma unroll 1
    for (int unit = wave; unit < 36; unit += 12) {
      const int mq = 1 + (unit >> 2), hh = unit & 3;
      const unsigned ub = 16 * (mq - 1) * QROW + 40 * hh;
      u32x2 q0 = *LDSP(u32x2, ub + qk_l + 16 * QROW), q1 = *LDSP(u32x2, ub + qk_l + 16 * QROW + 8);
      u32x2 k00 = *LDSP(u32x2, ub + qk_l + 160), k01 = *LDSP(u32x2, ub + qk_l + 168);
      u32x2 k10 = *LDSP(u32x2, ub + qk_l + 16 * QROW + 160), k11 = *LDSP(u32x2, ub + qk_l + 16 * QROW + 168);
      const bf16x8 vf0 = tr_frag(ub + v_l, ub + v_l + 16 * QROW);
      const bf16x8 vf1 = tr_frag(ub + v_l + 32, ub + v_l + 32 + 16 * QROW);
      if (q >= 2) { q1 = u32x2{0u, 0u}; }
      if (q == 3) { q0 = u32x2{0u, 0u}; }
      const bf16x8 qf = __builtin_bit_cast(bf16x8, u32x4{q0[0], q0[1], q1[0], q1[1]});
      const f32x4 z4 = {0.f, 0.f, 0.f, 0.f};
      const f32x4 s0 = __builtin_amdgcn_mfma_f32_16x16x32_bf16(__builtin_bit_cast(bf16x8, u32x4{k00[0], k00[1], k01[0], k01[1]}), qf, z4, 0, 0, 0);
      const f32x4 s1 = __builtin_amdgcn_mfma_f32_16x16x32_bf16(__builtin_bit_cast(bf16x8, u32x4{k10[0], k10[1], k11[0], k11[1]}), qf, z4, 0, 0, 0);
      float lg[8];
#pragma unroll
      for (int j = 0; j < 4; ++j) {
        lg[j] = __builtin_fmaf(s0[j], sl2, bias[j]);
        lg[4 + j] = __builtin_fmaf(s1[j], sl2, bias[4 + j]);
      }
      if (tok0 + 16 * (mq - 1) < 0) {
#pragma unroll
        for (int j = 0; j < 4; ++j) lg[j] = -3.0e38f;
      }
      float mx = fmaxf(fmaxf(fmaxf(lg[0], lg[1]), fmaxf(lg[2], lg[3])), fmaxf(fmaxf(lg[4], lg[5]), fmaxf(lg[6], lg[7])));
      mx = red_max4(mx);
      float sum = 0.f;
#pragma unroll
      for (int j = 0; j < 8; ++j) { lg[j] = __builtin_amdgcn_exp2f(lg[j] - mx); sum += lg[j]; }
      const float inv = __builtin_amdgcn_rcpf(red_sum4(sum));
      const bf16x8 pf = __builtin_bit_cast(bf16x8, u32x4{pack2(lg[0] * inv, lg[1] * inv), pack2(lg[2] * inv, lg[3] * inv),
                                                         pack2(lg[4] * inv, lg[5] * inv), pack2(lg[6] * inv, lg[7] * inv)});
      const f32x4 o0 = __builtin_amdgcn_mfma_f32_16x16x32_bf16(vf0, pf, z4, 0, 0, 0);
      const f32x4 o1 = __builtin_amdgcn_mfma_f32_16x16x32_bf16(vf1, pf, z4, 0, 0, 0);
      const int tokb = tok0 + 16 * mq;
      if (tokb < S_) {
        char* ob = a2 + (size_t)((tokb >> 6) * NKS2 * 4 + ((tokb >> 4) & 3)) * 1024 + c * 16;
        const int col0 = 80 * hg + 20 * hh + 4 * q;
        *(u32x2*)(ob + (col0 >> 5) * 4096 + ((col0 >> 2) & 3) * 256 + ((col0 >> 4) & 1) * 8) = u32x2{pack2(o0[0], o0[1]), pack2(o0[2], o0[3])};
        const int col1 = 80 * hg + 20 * hh + 16;
        if (q == 0)
          *(u32x2*)(ob + (col1 >> 5) * 4096 + ((col1 >> 2) & 3) * 256 + ((col1 >> 4) & 1) * 8) = u32x2{pack2(o1[0], o1[1]), pack2(o1[2], o1[3])};
      }
    }
  }
  STAMP(9);
  STAMP(10);
  STAMP_OUT(0);
}

constexpr int K2_DEPTH = 6;
__global__ __launch_bounds__(256, 1) void k2_kernel(const __hip_bfloat16* __restrict__ x,
                                                    char* __restrict__ ws,
                                                    __hip_bfloat16* __restrict__ out) {
  const int tid = threadIdx.x, lane = tid & 63;
  const int wave = __builtin_amdgcn_readfirstlane(tid >> 6);
  const int q = lane >> 4, c = lane & 15;
  const int xcd = blockIdx.x & 7, idx = blockIdx.x >> 3;
  const int rb = (xcd >> 1) * 8 + (idx >> 2), cb = (xcd & 1) * 4 + (idx & 3);
  STAMP_DECL;
  STAMP(0);

  const char* ap = ws + WS_A2 + (size_t)(rb * NKS2 + 17 * wave) * 4096 + lane * 16;
  const char* wp = ws + WS_W2 + (size_t)(cb * NKS2 + 17 * wave) * 5120 + lane * 16;

  bf16x8 fa[K2_DEPTH][4], fw[K2_DEPTH][5];
  f32x4 acc[4][5];
#pragma unroll
  for (int m = 0; m < 4; ++m)
#pragma unroll
    for (int n = 0; n < 5; ++n) acc[m][n] = f32x4{0.f, 0.f, 0.f, 0.f};

  auto ld = [&](int i) {
    const int s = i % K2_DEPTH;
#pragma unroll
    for (int m = 0; m < 4; ++m) fa[s][m] = *(const bf16x8*)(ap + i * 4096 + m * 1024);
#pragma unroll
    for (int n = 0; n < 5; ++n) fw[s][n] = *(const bf16x8*)(wp + i * 5120 + n * 1024);
  };
#pragma unroll
  for (int i = 0; i < K2_DEPTH; ++i) ld(i);
  __builtin_amdgcn_sched_barrier(0);
#pragma unroll
  for (int i = 0; i < 17; ++i) {
    const int s = i % K2_DEPTH;
#pragma unroll
    for (int m = 0; m < 4; ++m)
#pragma unroll
      for (int n = 0; n < 5; ++n)
        acc[m][n] = __builtin_amdgcn_mfma_f32_16x16x32_bf16(fw[s][n], fa[s][m], acc[m][n], 0, 0, 0);
    __builtin_amdgcn_sched_barrier(0);
    if (i + K2_DEPTH < 17) ld(i + K2_DEPTH);
    __builtin_amdgcn_sched_barrier(0);
  }

  const int tok = rb * 64 + wave * 16 + c;
  const size_t obase = (size_t)tok * D_ + cb * 80;
  const u32x4 x0 = *(const u32x4*)(x + obase + 8 * q);
  const u32x4 x1 = *(const u32x4*)(x + obase + 32 + 8 * q);
  const u32x2 x2 = *(const u32x2*)(x + obase + 64 + 4 * q);

#pragma unroll
  for (int m = 0; m < 4; ++m) {
    if (m != wave) {
      const int si = wave < m ? wave : wave - 1;
#pragma unroll
      for (int n = 0; n < 5; ++n) *LDSP(f32x4, ((m * 3 + si) * 5 + n) * 1024 + lane * 16) = acc[m][n];
    }
  }
  __syncthreads();
  f32x4 r[5];
#pragma unroll
  for (int n = 0; n < 5; ++n) {
    r[n] = wave == 0 ? acc[0][n] : (wave == 1 ? acc[1][n] : (wave == 2 ? acc[2][n] : acc[3][n]));
#pragma unroll
    for (int si = 0; si < 3; ++si) r[n] += *LDSP(f32x4, ((wave * 3 + si) * 5 + n) * 1024 + lane * 16);
  }
  auto lo = [](unsigned v) { return __builtin_bit_cast(float, v << 16); };
  auto hi = [](unsigned v) { return __builtin_bit_cast(float, v & 0xFFFF0000u); };
  u32x4 o0 = {pack2(r[0][0] + lo(x0[0]), r[0][1] + hi(x0[0])), pack2(r[0][2] + lo(x0[1]), r[0][3] + hi(x0[1])),
              pack2(r[1][0] + lo(x0[2]), r[1][1] + hi(x0[2])), pack2(r[1][2] + lo(x0[3]), r[1][3] + hi(x0[3]))};
  u32x4 o1 = {pack2(r[2][0] + lo(x1[0]), r[2][1] + hi(x1[0])), pack2(r[2][2] + lo(x1[1]), r[2][3] + hi(x1[1])),
              pack2(r[3][0] + lo(x1[2]), r[3][1] + hi(x1[2])), pack2(r[3][2] + lo(x1[3]), r[3][3] + hi(x1[3]))};
  u32x2 o2 = {pack2(r[4][0] + lo(x2[0]), r[4][1] + hi(x2[0])), pack2(r[4][2] + lo(x2[1]), r[4][3] + hi(x2[1]))};
  *(u32x4*)(out + obase + 8 * q) = o0;
  *(u32x4*)(out + obase + 32 + 8 * q) = o1;
  *(u32x2*)(out + obase + 64 + 4 * q) = o2;
  STAMP(6);
  STAMP_OUT(1);
}

extern "C" void kernel_launch(void* const* d_in, const int* in_sizes, int n_in,
                              void* d_out, int out_size, void* d_ws, size_t ws_size,
                              hipStream_t stream) {
  const __hip_bfloat16* x = (const __hip_bfloat16*)d_in[0];
  const __hip_bfloat16* wqkv = (const __hip_bfloat16*)d_in[1];
  const __hip_bfloat16* wo = (const __hip_bfloat16*)d_in[2];
  const __hip_bfloat16* wg = (const __hip_bfloat16*)d_in[4];
  const __hip_bfloat16* wu = (const __hip_bfloat16*)d_in[5];
  const __hip_bfloat16* wd = (const __hip_bfloat16*)d_in[6];
  (void)hipFuncSetAttribute((const void*)k1_kernel, hipFuncAttributeMaxDynamicSharedMemorySize, LDS_K1);
  k1_kernel<<<256, 768, LDS_K1, stream>>>(x, wqkv, wo, wg, wu, wd, (char*)d_ws);
  k2_kernel<<<256, 256, 61440, stream>>>(x, (char*)d_ws, (__hip_bfloat16*)d_out);
}
```

```cpp
#include <hip/hip_runtime.h>
#include <hip/hip_bf16.h>
#include <type_traits>

typedef short s16x4 __attribute__((ext_vector_type(4)));
typedef short s16x8 __attribute__((ext_vector_type(8)));
typedef __bf16 bf16x2 __attribute__((ext_vector_type(2)));
typedef __bf16 bf16x8 __attribute__((ext_vector_type(8)));
typedef float f32x2 __attribute__((ext_vector_type(2)));
typedef float f32x4 __attribute__((ext_vector_type(4)));
typedef unsigned u32x2 __attribute__((ext_vector_type(2)));
typedef unsigned u32x4 __attribute__((ext_vector_type(4)));
#define LDSP(T, a) ((__attribute__((address_space(3))) T*)(size_t)(unsigned)(a))

constexpr int S_ = 2048, D_ = 640, F_ = 1536;
constexpr int NKS2 = 68;
constexpr size_t WS_A2 = 0;
constexpr size_t WS_W2 = 9437184;

constexpr int A_SLOT = 24576;
constexpr int LDS_A = 0;
constexpr int B_SLOT = 16384;
constexpr int LDS_B = 3 * A_SLOT;
constexpr int LDS_ST = LDS_B + 5 * B_SLOT;
constexpr int LDS_CS = LDS_ST + 192 * 8;
constexpr int LDS_K1 = LDS_CS + 1024;
constexpr int QROW = 488;

__device__ __forceinline__ void dma16(const void* sbase, unsigned voff, unsigned lds_addr) {
  unsigned keep;
  asm volatile("s_mov_b32 %0, m0\n\ts_mov_b32 m0, %3\n\ts_nop 0\n\tglobal_load_lds_dwordx4 %1, %2\n\ts_mov_b32 m0, %0"
               : "=&s"(keep) : "v"(voff), "s"(sbase), "s"(lds_addr) : "memory");
}
template <int N> __device__ __forceinline__ void wait_vm() { asm volatile("s_waitcnt vmcnt(%0)" ::"i"(N) : "memory"); }
__device__ __forceinline__ void wait_lgkm0() {
  __builtin_amdgcn_s_waitcnt(0xC07F);
  asm volatile("" ::: "memory");
}

__device__ __forceinline__ bf16x8 tr_frag(unsigned a0, unsigned a1) {
  s16x4 lo = __builtin_amdgcn_ds_read_tr16_b64_v4i16(LDSP(s16x4, a0));
  s16x4 hi = __builtin_amdgcn_ds_read_tr16_b64_v4i16(LDSP(s16x4, a1));
  s16x8 v = __builtin_shufflevector(lo, hi, 0, 1, 2, 3, 4, 5, 6, 7);
  return __builtin_bit_cast(bf16x8, v);
}
__device__ __forceinline__ void mfma_acc(f32x4& c, const bf16x8& a, const bf16x8& b) {
  asm volatile("v_mfma_f32_16x16x32_bf16 %0, %1, %2, %0" : "+a"(c) : "v"(a), "v"(b));
}
__device__ __forceinline__ void mfma_accv(f32x4& c, const bf16x8& a, const bf16x8& b) {
  asm volatile("v_mfma_f32_16x16x32_bf16 %0, %1, %2, %0" : "+v"(c) : "v"(a), "v"(b));
}
__device__ __forceinline__ void st_wt(u32x4* p, u32x4 v) {
  asm volatile("global_store_dwordx4 %0, %1, off sc0 sc1\n\ts_nop 1" :: "v"(p), "v"(v) : "memory");
}
__device__ __forceinline__ void swap16(float& a, float& b) {
  asm volatile("s_nop 1\n\tv_permlane16_swap_b32 %0, %1" : "+v"(a), "+v"(b));
}
__device__ __forceinline__ void swap32(float& a, float& b) {
  asm volatile("s_nop 1\n\tv_permlane32_swap_b32 %0, %1" : "+v"(a), "+v"(b));
}
__device__ __forceinline__ float red_max4(float a) {
  float b = a;
  swap16(a, b); a = fmaxf(a, b); b = a;
  swap32(a, b); return fmaxf(a, b);
}
__device__ __forceinline__ float red_sum4(float a) {
  float b = a;
  swap16(a, b); a = a + b; b = a;
  swap32(a, b); return a + b;
}
__device__ __forceinline__ unsigned pack2(float a, float b) {
  bf16x2 p = {(__bf16)a, (__bf16)b};
  return __builtin_bit_cast(unsigned, p);
}

#define STAMP(i)
#define STAMP_DECL
#define STAMP_OUT(k)
template <int I, int N, class Fn> __device__ __forceinline__ void static_for(Fn&& f) {
  if constexpr (I < N) { f(std::integral_constant<int, I>{}); static_for<I + 1, N>(f); }
}

#define SB() __builtin_amdgcn_sched_barrier(0)
#define PC_BACK 0

__device__ __forceinline__ void retile_w2(char* ws, int job, int u0, int ustep, int lane) {
  const int q = lane >> 4, c = lane & 15;
  const int ks2 = job >> 1, half = job & 1;
  const int a = c >> 2, p = c & 3;
#pragma unroll 1
  for (int u = u0; u < 20; u += ustep) {
    const int cl = u / 5, n5 = u - 5 * cl;
    const int colst = n5 < 4 ? 32 * (n5 >> 1) + 8 * p + 4 * (n5 & 1) : 64 + 4 * p;
    const unsigned ad = LDS_A + 1 * A_SLOT + (4 * q + a) * 640 + (80 * cl + colst) * 2;
    bf16x8 f = tr_frag(ad, ad + 16 * 640);
    const int cb2 = 4 * half + cl;
    st_wt((u32x4*)(ws + WS_W2 + (size_t)((cb2 * NKS2 + ks2) * 5 + n5) * 1024 + lane * 16), __builtin_bit_cast(u32x4, f));
  }
}

__global__ __launch_bounds__(768, 3) void k1_kernel(const __hip_bfloat16* __restrict__ x,
                                                    const __hip_bfloat16* __restrict__ wqkv,
                                                    const __hip_bfloat16* __restrict__ wo,
                                                    const __hip_bfloat16* __restrict__ wg,
                                                    const __hip_bfloat16* __restrict__ wu,
                                                    const __hip_bfloat16* __restrict__ wd,
                                                    char* __restrict__ ws) {
  STAMP_DECL;
  STAMP(0);
  const int tid = threadIdx.x, lane = tid & 63;
  const int wave = __builtin_amdgcn_readfirstlane(tid >> 6);
  const int mh = (wave >> 2) & 1, wq = wave & 3;
  const int q = lane >> 4, c = lane & 15;

  const int xcd = blockIdx.x & 7, idx = blockIdx.x >> 3;
  const int xr = xcd >> 2, xc = xcd & 3;
  const int natt = xr == 0 ? 14 : 16;
  const bool is_att = idx < natt;
  int tok0, cbk = 0, hg = 0, job = 0;
  if (is_att) {
    const int ab = (xr == 0 ? 0 : 7) + (idx >> 1);
    hg = 2 * xc + (idx & 1);
    tok0 = 144 * ab - 16;
  } else {
    const int j = idx - natt;
    if (xr == 1 && j >= 15) {
      const int jb = 132 + xc;
      const char* wb = (const char*)wd + (size_t)((jb >> 1) - 20) * 32 * 1280 + 640 * (jb & 1);
      for (int pc = wave; pc < 20; pc += 12) {
        const int o = 1024 * pc + 16 * lane;
        const int row = o / 640;
        dma16(wb, (unsigned)(row * 1280 + (o - row * 640)), LDS_A + A_SLOT + pc * 1024);
      }
      wait_vm<0>();
      __builtin_amdgcn_s_barrier();
      retile_w2(ws, jb, wave, 12, lane);
      return;
    }
    const int rb = 6 * xr + j / 3;
    cbk = 3 * xc + j % 3;
    tok0 = 191 * rb - 1;
    job = rb * 12 + cbk;
  }
  const int mbase = is_att ? 5 * mh : 6 * mh;
  int ntile[4];
#pragma unroll
  for (int n = 0; n < 4; ++n) ntile[n] = is_att ? 4 * wq + n : (n < 2 ? 2 * wq + n : 8 + 2 * wq + n - 2);
  f32x4 acc[6][4];

  if (wave >= 8) {
    const int L = wq;
    unsigned b_voff[4];
    const char* b_base[2];
    int b_step;
#pragma unroll
    for (int j = 0; j < 4; ++j) {
      const int row = 4 * (L + 4 * (j & 1)) + (lane >> 4);
      const int hsw = (row & 3) | (((row >> 3) & 1) << 2);
      const int g = (lane & 15) ^ (hsw << 1);
      if (is_att) {
        int Lc = 16 * (j >> 1) + g;
        Lc = Lc > 29 ? 29 : Lc;
        const int sec = Lc / 10;
        b_voff[j] = (unsigned)(row * 3840 + (640 * sec + 80 * hg + 8 * (Lc - 10 * sec)) * 2);
      } else {
        b_voff[j] = (unsigned)(row * 3072 + (128 * cbk + 8 * g) * 2);
      }
    }
    if (is_att) { b_base[0] = (const char*)wqkv; b_base[1] = (const char*)wqkv; b_step = 32 * 3840; }
    else { b_base[0] = (const char*)wg; b_base[1] = (const char*)wu; b_step = 32 * 3072; }
    auto issueB = [&](int j, unsigned slot_off) {
#pragma unroll
      for (int e = 0; e < 4; ++e)
        dma16(b_base[e >> 1] + (size_t)j * b_step, b_voff[e], LDS_B + slot_off + (L + 4 * e) * 1024);
    };
    unsigned a_voff[6];
#pragma unroll
    for (int e = 0; e < 6; ++e) {
      const int ii = (e < 5 || !is_att) ? L + 4 * e : L + 16;
      const int row = 8 * ii + (lane >> 3);
      int tok = tok0 + row;
      tok = tok < 0 ? 0 : (tok > S_ - 1 ? S_ - 1 : tok);
      a_voff[e] = (unsigned)(tok * 1280 + (((lane & 7) ^ ((row >> 1) & 7)) << 4));
    }
    const unsigned a_dst5 = !is_att ? 20480u : 16384u;
    const bool a_on0 = L != 0 || lane >= 8;
    auto issueA = [&](int j, int h, unsigned slot_off) {
      const char* ab = (const char*)x + j * 128;
      const unsigned dst = LDS_A + slot_off + L * 1024;
      if (h == 0) {
        if (a_on0) dma16(ab, a_voff[0], dst);
        dma16(ab, a_voff[1], dst + 4096);
        dma16(ab, a_voff[2], dst + 8192);
      } else {
        dma16(ab, a_voff[3], dst + 12288);
        dma16(ab, a_voff[4], dst + 16384);
        dma16(ab, a_voff[5], dst + a_dst5);
      }
    };
    issueA(0, 0, 0); issueB(0, 0); issueA(0, 1, 0); issueB(1, B_SLOT); issueA(1, 0, A_SLOT); issueB(2, 2 * B_SLOT);
    if (tid - 512 < 24) {
      u32x4 ones = {0x3F803F80u, 0x3F803F80u, 0x3F803F80u, 0x3F803F80u};
      *LDSP(u32x4, LDS_A + ((tid - 512) >> 3) * A_SLOT + (tid & 7) * 16) = ones;
    }
    f32x4 gq[3], gs[3];
#pragma unroll
    for (int i = 0; i < 3; ++i) { gq[i] = f32x4{0.f, 0.f, 0.f, 0.f}; gs[i] = f32x4{0.f, 0.f, 0.f, 0.f}; }
    const bf16x8 ones8 = __builtin_bit_cast(bf16x8, u32x4{0x3F803F80u, 0x3F803F80u, 0x3F803F80u, 0x3F803F80u});
    const unsigned s_rd = LDS_A + L * 2048 + c * 128;
    bf16x8 sf[2][3];
    auto st_rd = [&](int sub, unsigned ao) {
      const unsigned ad = s_rd + ao + (((4 * sub + q) ^ ((c >> 1) & 7)) << 4);
#pragma unroll
      for (int i = 0; i < 3; ++i) sf[sub][i] = *LDSP(bf16x8, ad + i * 8192);
    };
    auto st_mma = [&](int sub) {
#pragma unroll
      for (int i = 0; i < 3; ++i) {
        gq[i] = __builtin_amdgcn_mfma_f32_16x16x32_bf16(sf[sub][i], sf[sub][i], gq[i], 0, 0, 0);
        gs[i] = __builtin_amdgcn_mfma_f32_16x16x32_bf16(ones8, sf[sub][i], gs[i], 0, 0, 0);
      }
    };
    auto issueJob = [&](int h) {
      const int ks = job >> 1, half = job & 1;
      const char* wb = ks < 20 ? (const char*)wo + (size_t)ks * 32 * 1280 : (const char*)wd + (size_t)(ks - 20) * 32 * 1280;
#pragma unroll
      for (int e = 3 * h; e < 3 * h + 3; ++e) {
        const int pe = e < 5 ? e : 4;
        const int o = 1024 * (L + 4 * pe) + 16 * lane;
        const int row = o / 640;
        dma16(wb, (unsigned)(row * 1280 + 640 * half + (o - row * 640)), LDS_A + A_SLOT + (L + 4 * pe) * 1024);
      }
    };
    wait_vm<11>();
    wait_lgkm0();
    __builtin_amdgcn_s_barrier();
    st_rd(0, 0);
    issueA(1, 1, A_SLOT); issueB(3, 3 * B_SLOT);
    unsigned bo = 0, ao = 0;
#pragma unroll 1
    for (int t = 0; t < 20; t += 2) {
      const unsigned bo1 = bo == 4 * B_SLOT ? 0 : bo + B_SLOT;
      const unsigned bo2 = bo1 == 4 * B_SLOT ? 0 : bo1 + B_SLOT;
      const unsigned ao1 = ao == 2 * A_SLOT ? 0 : ao + A_SLOT;
      const unsigned ao2 = ao1 == 2 * A_SLOT ? 0 : ao1 + A_SLOT;
      if (t == 0) wait_vm<14>(); else if (t <= 14) wait_vm<21>(); else if (t == 16) wait_vm<17>(); else wait_vm<6>();
      wait_lgkm0();
      __builtin_amdgcn_s_barrier();
      st_rd(1, ao);
      SB();
      if (t == 0) { issueA(2, 0, 2 * A_SLOT); issueB(4, 4 * B_SLOT); }
      if (t <= 14) { issueA((t >> 1) + 2, 1, ao2); issueB(t + 5, bo); }
      else if (t == 16) issueJob(1);
      SB();
      st_mma(0);
      SB();
      if (t <= 14) wait_vm<18>(); else if (t == 16) wait_vm<10>(); else wait_vm<0>();
      wait_lgkm0();
      __builtin_amdgcn_s_barrier();
      st_rd(0, ao1);
      SB();
      if (t <= 12) { issueA((t >> 1) + 3, 0, ao); issueB(t + 6, bo1); }
      else if (t == 14) issueJob(0);
      SB();
      st_mma(1);
      SB();
      bo = bo2; ao = ao1;
    }
#pragma unroll
    for (int i = 0; i < 3; ++i) {
      const int r = c & 3;
      const float b = r == 0 ? gq[i][0] : (r == 1 ? gq[i][1] : (r == 2 ? gq[i][2] : gq[i][3]));
      if (q == (c >> 2)) {
        const float mu = gs[i][0] * (1.0f / D_);
        float var = b * (1.0f / D_) - mu * mu;
        var = var < 0.f ? 0.f : var;
        const float rs = __builtin_amdgcn_rsqf(var + 1e-5f);
        *LDSP(f32x2, LDS_ST + ((L + 4 * i) * 16 + c) * 8) = f32x2{rs, -mu * rs};
      }
    }
#pragma unroll
    for (int m = 0; m < 6; ++m)
#pragma unroll
      for (int n = 0; n < 4; ++n) acc[m][n] = f32x4{0.f, 0.f, 0.f, 0.f};
  } else {
    unsigned code_pf = 0;
    if (wave < 6) {
      const char* pc = (const char*)(__builtin_amdgcn_s_getpc() & ~255ull);
      code_pf = *(const unsigned*)(pc - PC_BACK + wave * 4096 + lane * 64);
    }
    unsigned a_rd[2];
#pragma unroll
    for (int s = 0; s < 2; ++s)
      a_rd[s] = LDS_A + mbase * 2048 + c * 128 + (((4 * s + q) ^ ((c >> 1) & 7)) << 4);
    unsigned b_rd[4];
    {
      const int a = c >> 2, p = c & 3;
      const int hh = a | ((q & 1) << 2);
#pragma unroll
      for (int n = 0; n < 4; ++n)
        b_rd[n] = LDS_B + (ntile[n] >> 3) * 8192 + (8 * q + a) * 256 + (((ntile[n] & 7) ^ hh) << 5) + 8 * p;
    }
#pragma unroll
    for (int m = 0; m < 6; ++m)
#pragma unroll
      for (int n = 0; n < 4; ++n) {
        acc[m][n] = f32x4{0.f, 0.f, 0.f, 0.f};
        if (m < 5) asm volatile("" : "+a"(acc[m][n])); else asm volatile("s_nop 1" : "+v"(acc[m][n]));
      }
    bf16x8 af[6], bfr[2][4];
    auto step = [&](auto par, unsigned aon, unsigned bon) {
      constexpr int P = decltype(par)::value;
      wait_lgkm0();
      __builtin_amdgcn_s_barrier();
      SB();
      static_for<0, 6>([&](auto mc) {
        constexpr int m = decltype(mc)::value;
        if (m < 5 || !is_att) {
#pragma unroll
          for (int n = 0; n < 4; ++n) {
            if constexpr (m < 5) mfma_acc(acc[m][n], bfr[P][n], af[m]);
            else mfma_accv(acc[m][n], bfr[P][n], af[m]);
          }
        }
        SB();
        af[m] = *LDSP(bf16x8, a_rd[1 - P] + aon + m * 2048);
        if constexpr (m < 4) bfr[1 - P][m] = tr_frag(b_rd[m] + bon, b_rd[m] + bon + 1024);
        SB();
      });
    };
    using P0 = std::integral_constant<int, 0>;
    using P1 = std::integral_constant<int, 1>;
    __builtin_amdgcn_s_barrier();
#pragma unroll
    for (int n = 0; n < 4; ++n) bfr[0][n] = tr_frag(b_rd[n], b_rd[n] + 1024);
#pragma unroll
    for (int m = 0; m < 6; ++m) af[m] = *LDSP(bf16x8, a_rd[0] + m * 2048);
    STAMP(1);
    unsigned bo = 0, ao = 0;
#pragma unroll 1
    for (int t = 0; t < 20; t += 2) {
      const unsigned bo1 = bo == 4 * B_SLOT ? 0 : bo + B_SLOT;
      const unsigned bo2 = bo1 == 4 * B_SLOT ? 0 : bo1 + B_SLOT;
      const unsigned ao1 = ao == 2 * A_SLOT ? 0 : ao + A_SLOT;
      step(P0{}, ao, bo1);
      step(P1{}, ao1, bo2);
      bo = bo2; ao = ao1;
    }
    asm volatile("s_nop 15\n\ts_nop 15" ::: "memory");
    asm volatile("" :: "v"(code_pf));
    STAMP(6);
    if (mh == 0 && c == 0) {
#pragma unroll
      for (int n = 0; n < 4; ++n) *LDSP(f32x4, LDS_CS + (ntile[n] * 16 + 4 * q) * 4) = acc[0][n];
    }
  }
  wait_lgkm0();
  __builtin_amdgcn_s_barrier();

  f32x4 cs[4];
#pragma unroll
  for (int n = 0; n < 4; ++n) cs[n] = *LDSP(f32x4, LDS_CS + (ntile[n] * 16 + 4 * q) * 4);
  float rstd[6], nmr[6];
#pragma unroll
  for (int m = 0; m < 6; ++m) {
    const f32x2 v = *LDSP(f32x2, LDS_ST + ((mbase + m) * 16 + c) * 8);
    rstd[m] = v[0]; nmr[m] = v[1];
  }

  char* a2 = ws + WS_A2;
  STAMP(7);
  if (!is_att) {
    const int ks = 20 + 4 * cbk + wq;
    constexpr float NL2E = -1.4426950408889634f;
    if (wave < 8) {
#pragma unroll
      for (int m = 0; m < 6; ++m) {
        const int trow = (mbase + m) * 16 + c;
        const int tok = tok0 + trow;
        const float rg = rstd[m] * NL2E, ng = nmr[m] * NL2E;
        const f32x2 rg2 = {rg, rg}, ng2 = {ng, ng}, rs2 = {rstd[m], rstd[m]}, nm2 = {nmr[m], nmr[m]};
        unsigned pkv[4];
#pragma unroll
        for (int n = 0; n < 2; ++n)
#pragma unroll
          for (int h = 0; h < 2; ++h) {
            const f32x2 ag = {acc[m][n][2 * h], acc[m][n][2 * h + 1]}, au = {acc[m][n + 2][2 * h], acc[m][n + 2][2 * h + 1]};
            const f32x2 cg = {cs[n][2 * h], cs[n][2 * h + 1]}, cu = {cs[n + 2][2 * h], cs[n + 2][2 * h + 1]};
            const f32x2 eg = __builtin_elementwise_fma(ag, rg2, ng2 * cg);
            const f32x2 uu = __builtin_elementwise_fma(au, rs2, nm2 * cu);
            f32x2 d = {__builtin_amdgcn_exp2f(eg[0]), __builtin_amdgcn_exp2f(eg[1])};
            d = d + f32x2{1.0f, 1.0f};
            const f32x2 hv = uu * f32x2{__builtin_amdgcn_rcpf(d[0]), __builtin_amdgcn_rcpf(d[1])};
            pkv[2 * n + h] = pack2(hv[0], hv[1]);
          }
        if (trow >= 1 && tok < S_)
          st_wt((u32x4*)(a2 + (size_t)(((tok >> 6) * NKS2 + ks) * 4 + ((tok >> 4) & 3)) * 1024 + (16 * q + (tok & 15)) * 16),
                u32x4{pkv[0], pkv[1], pkv[2], pkv[3]});
      }
    } else {
      retile_w2(ws, job, wq, 4, lane);
    }
  } else {
    if (wave < 8) {
#pragma unroll
      for (int m = 0; m < 5; ++m) {
        const int trow = (mbase + m) * 16 + c;
        const f32x2 rs2 = {rstd[m], rstd[m]}, nm2 = {nmr[m], nmr[m]};
#pragma unroll
        for (int n = 0; n < 4; ++n) {
          const f32x2 y0 = __builtin_elementwise_fma(f32x2{acc[m][n][0], acc[m][n][1]}, rs2, nm2 * f32x2{cs[n][0], cs[n][1]});
          const f32x2 y1 = __builtin_elementwise_fma(f32x2{acc[m][n][2], acc[m][n][3]}, rs2, nm2 * f32x2{cs[n][2], cs[n][3]});
          if (n < 3 || wq < 3)
            *LDSP(u32x2, trow * QROW + (ntile[n] * 16 + 4 * q) * 2) = u32x2{pack2(y0[0], y0[1]), pack2(y1[0], y1[1])};
        }
      }
    }
    float bias[8];
#pragma unroll
    for (int j = 0; j < 8; ++j) {
      const int diff = 16 + c - (16 * (j >> 2) + 4 * q + (j & 3));
      bias[j] = (diff >= 0 && diff <= 14 && (diff & 1) == 0) ? 0.f : -3.0e38f;
    }
    const float sl2 = 0.22360679774997896f * 1.4426950408889634f;
    const int dofs = q < 2 ? 16 * q : 32;
    const unsigned qk_l = c * QROW + dofs;
    const unsigned v_l = (4 * q + (c >> 2)) * QROW + 320 + 8 * (c & 3);
    wait_lgkm0();
    __builtin_amdgcn_s_barrier();
    STAMP(8);
#pragma unroll 1
    for (int unit = wave; unit < 36; unit += 12) {
      const int mq = 1 + (unit >> 2), hh = unit & 3;
      const unsigned ub = 16 * (mq - 1) * QROW + 40 * hh;
      u32x2 q0 = *LDSP(u32x2, ub + qk_l + 16 * QROW), q1 = *LDSP(u32x2, ub + qk_l + 16 * QROW + 8);
      u32x2 k00 = *LDSP(u32x2, ub + qk_l + 160), k01 = *LDSP(u32x2, ub + qk_l + 168);
      u32x2 k10 = *LDSP(u32x2, ub + qk_l + 16 * QROW + 160), k11 = *LDSP(u32x2, ub + qk_l + 16 * QROW + 168);
      const bf16x8 vf0 = tr_frag(ub + v_l, ub + v_l + 16 * QROW);
      const bf16x8 vf1 = tr_frag(ub + v_l + 32, ub + v_l + 32 + 16 * QROW);
      if (q >= 2) { q1 = u32x2{0u, 0u}; }
      if (q == 3) { q0 = u32x2{0u, 0u}; }
      const bf16x8 qf = __builtin_bit_cast(bf16x8, u32x4{q0[0], q0[1], q1[0], q1[1]});
      const f32x4 z4 = {0.f, 0.f, 0.f, 0.f};
      const f32x4 s0 = __builtin_amdgcn_mfma_f32_16x16x32_bf16(__builtin_bit_cast(bf16x8, u32x4{k00[0], k00[1], k01[0], k01[1]}), qf, z4, 0, 0, 0);
      const f32x4 s1 = __builtin_amdgcn_mfma_f32_16x16x32_bf16(__builtin_bit_cast(bf16x8, u32x4{k10[0], k10[1], k11[0], k11[1]}), qf, z4, 0, 0, 0);
      float lg[8];
#pragma unroll
      for (int j = 0; j < 4; ++j) {
        lg[j] = __builtin_fmaf(s0[j], sl2, bias[j]);
        lg[4 + j] = __builtin_fmaf(s1[j], sl2, bias[4 + j]);
      }
      if (tok0 + 16 * (mq - 1) < 0) {
#pragma unroll
        for (int j = 0; j < 4; ++j) lg[j] = -3.0e38f;
      }
      float mx = fmaxf(fmaxf(fmaxf(lg[0], lg[1]), fmaxf(lg[2], lg[3])), fmaxf(fmaxf(lg[4], lg[5]), fmaxf(lg[6], lg[7])));
      mx = red_max4(mx);
      float sum = 0.f;
#pragma unroll
      for (int j = 0; j < 8; ++j) { lg[j] = __builtin_amdgcn_exp2f(lg[j] - mx); sum += lg[j]; }
      const float inv = __builtin_amdgcn_rcpf(red_sum4(sum));
      const bf16x8 pf = __builtin_bit_cast(bf16x8, u32x4{pack2(lg[0] * inv, lg[1] * inv), pack2(lg[2] * inv, lg[3] * inv),
                                                         pack2(lg[4] * inv, lg[5] * inv), pack2(lg[6] * inv, lg[7] * inv)});
      const f32x4 o0 = __builtin_amdgcn_mfma_f32_16x16x32_bf16(vf0, pf, z4, 0, 0, 0);
      const f32x4 o1 = __builtin_amdgcn_mfma_f32_16x16x32_bf16(vf1, pf, z4, 0, 0, 0);
      const int tokb = tok0 + 16 * mq;
      if (tokb < S_) {
        char* ob = a2 + (size_t)((tokb >> 6) * NKS2 * 4 + ((tokb >> 4) & 3)) * 1024 + c * 16;
        const int col0 = 80 * hg + 20 * hh + 4 * q;
        *(u32x2*)(ob + (col0 >> 5) * 4096 + ((col0 >> 2) & 3) * 256 + ((col0 >> 4) & 1) * 8) = u32x2{pack2(o0[0], o0[1]), pack2(o0[2], o0[3])};
        const int col1 = 80 * hg + 20 * hh + 16;
        if (q == 0)
          *(u32x2*)(ob + (col1 >> 5) * 4096 + ((col1 >> 2) & 3) * 256 + ((col1 >> 4) & 1) * 8) = u32x2{pack2(o1[0], o1[1]), pack2(o1[2], o1[3])};
      }
    }
  }
  STAMP(9);
  STAMP(10);
  STAMP_OUT(0);
}

constexpr int K2_DEPTH = 6;
__global__ __launch_bounds__(256, 1) void k2_kernel(const __hip_bfloat16* __restrict__ x,
                                                    char* __restrict__ ws,
                                                    __hip_bfloat16* __restrict__ out) {
  const int tid = threadIdx.x, lane = tid & 63;
  const int wave = __builtin_amdgcn_readfirstlane(tid >> 6);
  const int q = lane >> 4, c = lane & 15;
  const int xcd = blockIdx.x & 7, idx = blockIdx.x >> 3;
  const int rb = (xcd >> 1) * 8 + (idx >> 2), cb = (xcd & 1) * 4 + (idx & 3);
  STAMP_DECL;
  STAMP(0);

  const char* ap = ws + WS_A2 + (size_t)(rb * NKS2 + 17 * wave) * 4096 + lane * 16;
  const char* wp = ws + WS_W2 + (size_t)(cb * NKS2 + 17 * wave) * 5120 + lane * 16;

  bf16x8 fa[K2_DEPTH][4], fw[K2_DEPTH][5];
  f32x4 acc[4][5];
#pragma unroll
  for (int m = 0; m < 4; ++m)
#pragma unroll
    for (int n = 0; n < 5; ++n) acc[m][n] = f32x4{0.f, 0.f, 0.f, 0.f};

  auto ld = [&](int i) {
    const int s = i % K2_DEPTH;
#pragma unroll
    for (int m = 0; m < 4; ++m) fa[s][m] = *(const bf16x8*)(ap + i * 4096 + m * 1024);
#pragma unroll
    for (int n = 0; n < 5; ++n) fw[s][n] = *(const bf16x8*)(wp + i * 5120 + n * 1024);
  };
#pragma unroll
  for (int i = 0; i < K2_DEPTH; ++i) ld(i);
  __builtin_amdgcn_sched_barrier(0);
#pragma unroll
  for (int i = 0; i < 17; ++i) {
    const int s = i % K2_DEPTH;
#pragma unroll
    for (int m = 0; m < 4; ++m)
#pragma unroll
      for (int n = 0; n < 5; ++n)
        acc[m][n] = __builtin_amdgcn_mfma_f32_16x16x32_bf16(fw[s][n], fa[s][m], acc[m][n], 0, 0, 0);
    __builtin_amdgcn_sched_barrier(0);
    if (i + K2_DEPTH < 17) ld(i + K2_DEPTH);
    __builtin_amdgcn_sched_barrier(0);
  }

  const int tok = rb * 64 + wave * 16 + c;
  const size_t obase = (size_t)tok * D_ + cb * 80;
  const u32x4 x0 = *(const u32x4*)(x + obase + 8 * q);
  const u32x4 x1 = *(const u32x4*)(x + obase + 32 + 8 * q);
  const u32x2 x2 = *(const u32x2*)(x + obase + 64 + 4 * q);

#pragma unroll
  for (int m = 0; m < 4; ++m) {
    if (m != wave) {
      const int si = wave < m ? wave : wave - 1;
#pragma unroll
      for (int n = 0; n < 5; ++n) *LDSP(f32x4, ((m * 3 + si) * 5 + n) * 1024 + lane * 16) = acc[m][n];
    }
  }
  __syncthreads();
  f32x4 r[5];
#pragma unroll
  for (int n = 0; n < 5; ++n) {
    r[n] = wave == 0 ? acc[0][n] : (wave == 1 ? acc[1][n] : (wave == 2 ? acc[2][n] : acc[3][n]));
#pragma unroll
    for (int si = 0; si < 3; ++si) r[n] += *LDSP(f32x4, ((wave * 3 + si) * 5 + n) * 1024 + lane * 16);
  }
  auto lo = [](unsigned v) { return __builtin_bit_cast(float, v << 16); };
  auto hi = [](unsigned v) { return __builtin_bit_cast(float, v & 0xFFFF0000u); };
  u32x4 o0 = {pack2(r[0][0] + lo(x0[0]), r[0][1] + hi(x0[0])), pack2(r[0][2] + lo(x0[1]), r[0][3] + hi(x0[1])),
              pack2(r[1][0] + lo(x0[2]), r[1][1] + hi(x0[2])), pack2(r[1][2] + lo(x0[3]), r[1][3] + hi(x0[3]))};
  u32x4 o1 = {pack2(r[2][0] + lo(x1[0]), r[2][1] + hi(x1[0])), pack2(r[2][2] + lo(x1[1]), r[2][3] + hi(x1[1])),
              pack2(r[3][0] + lo(x1[2]), r[3][1] + hi(x1[2])), pack2(r[3][2] + lo(x1[3]), r[3][3] + hi(x1[3]))};
  u32x2 o2 = {pack2(r[4][0] + lo(x2[0]), r[4][1] + hi(x2[0])), pack2(r[4][2] + lo(x2[1]), r[4][3] + hi(x2[1]))};
  *(u32x4*)(out + obase + 8 * q) = o0;
  *(u32x4*)(out + obase + 32 + 8 * q) = o1;
  *(u32x2*)(out + obase + 64 + 4 * q) = o2;
  STAMP(6);
  STAMP_OUT(1);
}

extern "C" void kernel_launch(void* const* d_in, const int* in_sizes, int n_in,
                              void* d_out, int out_size, void* d_ws, size_t ws_size,
                              hipStream_t stream) {
  const __hip_bfloat16* x = (const __hip_bfloat16*)d_in[0];
  const __hip_bfloat16* wqkv = (const __hip_bfloat16*)d_in[1];
  const __hip_bfloat16* wo = (const __hip_bfloat16*)d_in[2];
  const __hip_bfloat16* wg = (const __hip_bfloat16*)d_in[4];
  const __hip_bfloat16* wu = (const __hip_bfloat16*)d_in[5];
  const __hip_bfloat16* wd = (const __hip_bfloat16*)d_in[6];
  (void)hipFuncSetAttribute((const void*)k1_kernel, hipFuncAttributeMaxDynamicSharedMemorySize, LDS_K1);
  k1_kernel<<<256, 768, LDS_K1, stream>>>(x, wqkv, wo, wg, wu, wd, (char*)d_ws);
  k2_kernel<<<256, 256, 61440, stream>>>(x, (char*)d_ws, (__hip_bfloat16*)d_out);
}
```

```cpp
#include <hip/hip_runtime.h>
#include <hip/hip_bf16.h>
#include <type_traits>

typedef short s16x4 __attribute__((ext_vector_type(4)));
typedef short s16x8 __attribute__((ext_vector_type(8)));
typedef __bf16 bf16x2 __attribute__((ext_vector_type(2)));
typedef __bf16 bf16x8 __attribute__((ext_vector_type(8)));
typedef float f32x2 __attribute__((ext_vector_type(2)));
typedef float f32x4 __attribute__((ext_vector_type(4)));
typedef unsigned u32x2 __attribute__((ext_vector_type(2)));
typedef unsigned u32x4 __attribute__((ext_vector_type(4)));
#define LDSP(T, a) ((__attribute__((address_space(3))) T*)(size_t)(unsigned)(a))

constexpr int S_ = 2048, D_ = 640, F_ = 1536;
constexpr int NKS2 = 68;
constexpr size_t WS_A2 = 0;
constexpr size_t WS_W2 = 9437184;

constexpr int A_SLOT = 24576;
constexpr int LDS_A = 0;
constexpr int B_SLOT = 16384;
constexpr int LDS_B = 3 * A_SLOT;
constexpr int LDS_ST = LDS_B + 5 * B_SLOT;
constexpr int LDS_CS = LDS_ST + 192 * 8;
constexpr int LDS_ZERO = LDS_CS + 1024;
constexpr int LDS_K1 = LDS_ZERO + 16;
constexpr int QROW = 488;

__device__ __forceinline__ void dma16(const void* sbase, unsigned voff, unsigned lds_addr) {
  unsigned keep;
  asm volatile("s_mov_b32 %0, m0\n\ts_mov_b32 m0, %3\n\ts_nop 0\n\tglobal_load_lds_dwordx4 %1, %2\n\ts_mov_b32 m0, %0"
               : "=&s"(keep) : "v"(voff), "s"(sbase), "s"(lds_addr) : "memory");
}
template <int N> __device__ __forceinline__ void wait_vm() { asm volatile("s_waitcnt vmcnt(%0)" ::"i"(N) : "memory"); }
__device__ __forceinline__ void wait_lgkm0() {
  __builtin_amdgcn_s_waitcnt(0xC07F);
  asm volatile("" ::: "memory");
}

__device__ __forceinline__ bf16x8 tr_frag(unsigned a0, unsigned a1) {
  s16x4 lo = __builtin_amdgcn_ds_read_tr16_b64_v4i16(LDSP(s16x4, a0));
  s16x4 hi = __builtin_amdgcn_ds_read_tr16_b64_v4i16(LDSP(s16x4, a1));
  s16x8 v = __builtin_shufflevector(lo, hi, 0, 1, 2, 3, 4, 5, 6, 7);
  return __builtin_bit_cast(bf16x8, v);
}
__device__ __forceinline__ void mfma_acc(f32x4& c, const bf16x8& a, const bf16x8& b) {
  asm volatile("v_mfma_f32_16x16x32_bf16 %0, %1, %2, %0" : "+a"(c) : "v"(a), "v"(b));
}
__device__ __forceinline__ void mfma_accv(f32x4& c, const bf16x8& a, const bf16x8& b) {
  asm volatile("v_mfma_f32_16x16x32_bf16 %0, %1, %2, %0" : "+v"(c) : "v"(a), "v"(b));
}
__device__ __forceinline__ void st_wt(u32x4* p, u32x4 v) {
  asm volatile("global_store_dwordx4 %0, %1, off sc0 sc1\n\ts_nop 1" :: "v"(p), "v"(v) : "memory");
}
__device__ __forceinline__ void swap16(float& a, float& b) {
  asm volatile("s_nop 1\n\tv_permlane16_swap_b32 %0, %1" : "+v"(a), "+v"(b));
}
__device__ __forceinline__ void swap32(float& a, float& b) {
  asm volatile("s_nop 1\n\tv_permlane32_swap_b32 %0, %1" : "+v"(a), "+v"(b));
}
__device__ __forceinline__ float red_max4(float a) {
  float b = a;
  swap16(a, b); a = fmaxf(a, b); b = a;
  swap32(a, b); return fmaxf(a, b);
}
__device__ __forceinline__ float red_sum4(float a) {
  float b = a;
  swap16(a, b); a = a + b; b = a;
  swap32(a, b); return a + b;
}
__device__ __forceinline__ unsigned pack2(float a, float b) {
  bf16x2 p = {(__bf16)a, (__bf16)b};
  return __builtin_bit_cast(unsigned, p);
}

#define STAMP(i)
#define STAMP_DECL
#define STAMP_OUT(k)
template <int I, int N, class Fn> __device__ __forceinline__ void static_for(Fn&& f) {
  if constexpr (I < N) { f(std::integral_constant<int, I>{}); static_for<I + 1, N>(f); }
}

#define SB() __builtin_amdgcn_sched_barrier(0)
#define PC_BACK 0

__device__ __forceinline__ void retile_w2(char* ws, int job, int u0, int ustep, int lane) {
  const int q = lane >> 4, c = lane & 15;
  const int ks2 = job >> 1, half = job & 1;
  const int a = c >> 2, p = c & 3;
#pragma unroll 1
  for (int u = u0; u < 20; u += ustep) {
    const int cl = u / 5, n5 = u - 5 * cl;
    const int colst = n5 < 4 ? 32 * (n5 >> 1) + 8 * p + 4 * (n5 & 1) : 64 + 4 * p;
    const unsigned ad = LDS_A + 1 * A_SLOT + (4 * q + a) * 640 + (80 * cl + colst) * 2;
    bf16x8 f = tr_frag(ad, ad + 16 * 640);
    const int cb2 = 4 * half + cl;
    st_wt((u32x4*)(ws + WS_W2 + (size_t)((cb2 * NKS2 + ks2) * 5 + n5) * 1024 + lane * 16), __builtin_bit_cast(u32x4, f));
  }
}

__global__ __launch_bounds__(768, 3) void k1_kernel(const __hip_bfloat16* __restrict__ x,
                                                    const __hip_bfloat16* __restrict__ wqkv,
                                                    const __hip_bfloat16* __restrict__ wo,
                                                    const __hip_bfloat16* __restrict__ wg,
                                                    const __hip_bfloat16* __restrict__ wu,
                                                    const __hip_bfloat16* __restrict__ wd,
                                                    char* __restrict__ ws) {
  STAMP_DECL;
  STAMP(0);
  const int tid = threadIdx.x, lane = tid & 63;
  const int wave = __builtin_amdgcn_readfirstlane(tid >> 6);
  const int mh = (wave >> 2) & 1, wq = wave & 3;
  const int q = lane >> 4, c = lane & 15;

  const int xcd = blockIdx.x & 7, idx = blockIdx.x >> 3;
  const int xr = xcd >> 2, xc = xcd & 3;
  const int natt = xr == 0 ? 14 : 16;
  const bool is_att = idx < natt;
  int tok0, cbk = 0, hg = 0, job = 0;
  if (is_att) {
    const int ab = (xr == 0 ? 0 : 7) + (idx >> 1);
    hg = 2 * xc + (idx & 1);
    tok0 = 144 * ab - 16;
  } else {
    const int j = idx - natt;
    if (xr == 1 && j >= 15) {
      const int jb = 132 + xc;
      const char* wb = (const char*)wd + (size_t)((jb >> 1) - 20) * 32 * 1280 + 640 * (jb & 1);
      for (int pc = wave; pc < 20; pc += 12) {
        const int o = 1024 * pc + 16 * lane;
        const int row = o / 640;
        dma16(wb, (unsigned)(row * 1280 + (o - row * 640)), LDS_A + A_SLOT + pc * 1024);
      }
      wait_vm<0>();
      __builtin_amdgcn_s_barrier();
      retile_w2(ws, jb, wave, 12, lane);
      return;
    }
    const int rb = 6 * xr + j / 3;
    cbk = 3 * xc + j % 3;
    tok0 = 191 * rb - 1;
    job = rb * 12 + cbk;
  }
  const int mbase = is_att ? 5 * mh : 6 * mh;
  int ntile[4];
#pragma unroll
  for (int n = 0; n < 4; ++n) ntile[n] = is_att ? 4 * wq + n : (n < 2 ? 2 * wq + n : 8 + 2 * wq + n - 2);
  f32x4 acc[6][4];

  if (wave >= 8) {
    const int L = wq;
    unsigned b_voff[4];
    const char* b_base[2];
    int b_step;
#pragma unroll
    for (int j = 0; j < 4; ++j) {
      const int row = 4 * (L + 4 * (j & 1)) + (lane >> 4);
      const int hsw = (row & 3) | (((row >> 3) & 1) << 2);
      const int g = (lane & 15) ^ (hsw << 1);
      if (is_att) {
        int Lc = 16 * (j >> 1) + g;
        Lc = Lc > 29 ? 29 : Lc;
        const int sec = Lc / 10;
        b_voff[j] = (unsigned)(row * 3840 + (640 * sec + 80 * hg + 8 * (Lc - 10 * sec)) * 2);
      } else {
        b_voff[j] = (unsigned)(row * 3072 + (128 * cbk + 8 * g) * 2);
      }
    }
    if (is_att) { b_base[0] = (const char*)wqkv; b_base[1] = (const char*)wqkv; b_step = 32 * 3840; }
    else { b_base[0] = (const char*)wg; b_base[1] = (const char*)wu; b_step = 32 * 3072; }
    auto issueB = [&](int j, unsigned slot_off) {
#pragma unroll
      for (int e = 0; e < 4; ++e)
        dma16(b_base[e >> 1] + (size_t)j * b_step, b_voff[e], LDS_B + slot_off + (L + 4 * e) * 1024);
    };
    unsigned a_voff[6];
#pragma unroll
    for (int e = 0; e < 6; ++e) {
      const int ii = (e < 5 || !is_att) ? L + 4 * e : L + 16;
      const int row = 8 * ii + (lane >> 3);
      int tok = tok0 + row;
      tok = tok < 0 ? 0 : (tok > S_ - 1 ? S_ - 1 : tok);
      a_voff[e] = (unsigned)(tok * 1280 + (((lane & 7) ^ ((row >> 1) & 7)) << 4));
    }
    const unsigned a_dst5 = !is_att ? 20480u : 16384u;
    const bool a_on0 = L != 0 || lane >= 8;
    auto issueA = [&](int j, int h, unsigned slot_off) {
      const char* ab = (const char*)x + j * 128;
      const unsigned dst = LDS_A + slot_off + L * 1024;
      if (h == 0) {
        if (a_on0) dma16(ab, a_voff[0], dst);
        dma16(ab, a_voff[1], dst + 4096);
        dma16(ab, a_voff[2], dst + 8192);
      } else {
        dma16(ab, a_voff[3], dst + 12288);
        dma16(ab, a_voff[4], dst + 16384);
        dma16(ab, a_voff[5], dst + a_dst5);
      }
    };
    issueA(0, 0, 0); issueB(0, 0); issueA(0, 1, 0); issueB(1, B_SLOT); issueA(1, 0, A_SLOT); issueB(2, 2 * B_SLOT);
    if (tid - 512 < 24) {
      u32x4 ones = {0x3F803F80u, 0x3F803F80u, 0x3F803F80u, 0x3F803F80u};
      *LDSP(u32x4, LDS_A + ((tid - 512) >> 3) * A_SLOT + (tid & 7) * 16) = ones;
    }
    f32x4 gq[3], gs[3];
#pragma unroll
    for (int i = 0; i < 3; ++i) { gq[i] = f32x4{0.f, 0.f, 0.f, 0.f}; gs[i] = f32x4{0.f, 0.f, 0.f, 0.f}; }
    const bf16x8 ones8 = __builtin_bit_cast(bf16x8, u32x4{0x3F803F80u, 0x3F803F80u, 0x3F803F80u, 0x3F803F80u});
    const unsigned s_rd = LDS_A + L * 2048 + c * 128;
    bf16x8 sf[2][3];
    auto st_rd = [&](int sub, unsigned ao) {
      const unsigned ad = s_rd + ao + (((4 * sub + q) ^ ((c >> 1) & 7)) << 4);
#pragma unroll
      for (int i = 0; i < 3; ++i) sf[sub][i] = *LDSP(bf16x8, ad + i * 8192);
    };
    auto st_mma = [&](int sub) {
#pragma unroll
      for (int i = 0; i < 3; ++i) {
        gq[i] = __builtin_amdgcn_mfma_f32_16x16x32_bf16(sf[sub][i], sf[sub][i], gq[i], 0, 0, 0);
        gs[i] = __builtin_amdgcn_mfma_f32_16x16x32_bf16(ones8, sf[sub][i], gs[i], 0, 0, 0);
      }
    };
    auto issueJob = [&](int h) {
      const int ks = job >> 1, half = job & 1;
      const char* wb = ks < 20 ? (const char*)wo + (size_t)ks * 32 * 1280 : (const char*)wd + (size_t)(ks - 20) * 32 * 1280;
#pragma unroll
      for (int e = 3 * h; e < 3 * h + 3; ++e) {
        const int pe = e < 5 ? e : 4;
        const int o = 1024 * (L + 4 * pe) + 16 * lane;
        const int row = o / 640;
        dma16(wb, (unsigned)(row * 1280 + 640 * half + (o - row * 640)), LDS_A + A_SLOT + (L + 4 * pe) * 1024);
      }
    };
    wait_vm<11>();
    wait_lgkm0();
    __builtin_amdgcn_s_barrier();
    st_rd(0, 0);
    issueA(1, 1, A_SLOT); issueB(3, 3 * B_SLOT);
    unsigned bo = 0, ao = 0;
#pragma unroll 1
    for (int t = 0; t < 20; t += 2) {
      const unsigned bo1 = bo == 4 * B_SLOT ? 0 : bo + B_SLOT;
      const unsigned bo2 = bo1 == 4 * B_SLOT ? 0 : bo1 + B_SLOT;
      const unsigned ao1 = ao == 2 * A_SLOT ? 0 : ao + A_SLOT;
      const unsigned ao2 = ao1 == 2 * A_SLOT ? 0 : ao1 + A_SLOT;
      if (t == 0) wait_vm<14>(); else if (t <= 14) wait_vm<21>(); else if (t == 16) wait_vm<17>(); else wait_vm<6>();
      wait_lgkm0();
      __builtin_amdgcn_s_barrier();
      st_rd(1, ao);
      SB();
      if (t == 0) { issueA(2, 0, 2 * A_SLOT); issueB(4, 4 * B_SLOT); }
      if (t <= 14) { issueA((t >> 1) + 2, 1, ao2); issueB(t + 5, bo); }
      else if (t == 16) issueJob(1);
      SB();
      st_mma(0);
      SB();
      if (t <= 14) wait_vm<18>(); else if (t == 16) wait_vm<10>(); else wait_vm<0>();
      wait_lgkm0();
      __builtin_amdgcn_s_barrier();
      st_rd(0, ao1);
      SB();
      if (t <= 12) { issueA((t >> 1) + 3, 0, ao); issueB(t + 6, bo1); }
      else if (t == 14) issueJob(0);
      SB();
      st_mma(1);
      SB();
      bo = bo2; ao = ao1;
    }
#pragma unroll
    for (int i = 0; i < 3; ++i) {
      const int r = c & 3;
      const float b = r == 0 ? gq[i][0] : (r == 1 ? gq[i][1] : (r == 2 ? gq[i][2] : gq[i][3]));
      if (q == (c >> 2)) {
        const float mu = gs[i][0] * (1.0f / D_);
        float var = b * (1.0f / D_) - mu * mu;
        var = var < 0.f ? 0.f : var;
        const float rs = __builtin_amdgcn_rsqf(var + 1e-5f);
        *LDSP(f32x2, LDS_ST + ((L + 4 * i) * 16 + c) * 8) = f32x2{rs, -mu * rs};
      }
    }
#pragma unroll
    for (int m = 0; m < 6; ++m)
#pragma unroll
      for (int n = 0; n < 4; ++n) acc[m][n] = f32x4{0.f, 0.f, 0.f, 0.f};
  } else {
    unsigned code_pf = 0;
    if (wave < 6) {
      const char* pc = (const char*)(__builtin_amdgcn_s_getpc() & ~255ull);
      code_pf = *(const unsigned*)(pc - PC_BACK + wave * 4096 + lane * 64);
    }
    unsigned a_rd[2];
#pragma unroll
    for (int s = 0; s < 2; ++s)
      a_rd[s] = LDS_A + mbase * 2048 + c * 128 + (((4 * s + q) ^ ((c >> 1) & 7)) << 4);
    unsigned b_rd[4];
    {
      const int a = c >> 2, p = c & 3;
      const int hh = a | ((q & 1) << 2);
#pragma unroll
      for (int n = 0; n < 4; ++n)
        b_rd[n] = LDS_B + (ntile[n] >> 3) * 8192 + (8 * q + a) * 256 + (((ntile[n] & 7) ^ hh) << 5) + 8 * p;
    }
#pragma unroll
    for (int m = 0; m < 6; ++m)
#pragma unroll
      for (int n = 0; n < 4; ++n) {
        acc[m][n] = f32x4{0.f, 0.f, 0.f, 0.f};
        if (m < 5) asm volatile("" : "+a"(acc[m][n])); else asm volatile("s_nop 1" : "+v"(acc[m][n]));
      }
    bf16x8 af[6], bfr[2][4];
    auto step = [&](auto par, unsigned aon, unsigned bon) {
      constexpr int P = decltype(par)::value;
      wait_lgkm0();
      __builtin_amdgcn_s_barrier();
      SB();
      static_for<0, 6>([&](auto mc) {
        constexpr int m = decltype(mc)::value;
        if (m < 5 || !is_att) {
#pragma unroll
          for (int n = 0; n < 4; ++n) {
            if constexpr (m < 5) mfma_acc(acc[m][n], bfr[P][n], af[m]);
            else mfma_accv(acc[m][n], bfr[P][n], af[m]);
          }
        }
        SB();
        af[m] = *LDSP(bf16x8, a_rd[1 - P] + aon + m * 2048);
        if constexpr (m < 4) bfr[1 - P][m] = tr_frag(b_rd[m] + bon, b_rd[m] + bon + 1024);
        SB();
      });
    };
    using P0 = std::integral_constant<int, 0>;
    using P1 = std::integral_constant<int, 1>;
    __builtin_amdgcn_s_barrier();
#pragma unroll
    for (int n = 0; n < 4; ++n) bfr[0][n] = tr_frag(b_rd[n], b_rd[n] + 1024);
#pragma unroll
    for (int m = 0; m < 6; ++m) af[m] = *LDSP(bf16x8, a_rd[0] + m * 2048);
    STAMP(1);
    unsigned bo = 0, ao = 0;
#pragma unroll 1
    for (int t = 0; t < 20; t += 2) {
      const unsigned bo1 = bo == 4 * B_SLOT ? 0 : bo + B_SLOT;
      const unsigned bo2 = bo1 == 4 * B_SLOT ? 0 : bo1 + B_SLOT;
      const unsigned ao1 = ao == 2 * A_SLOT ? 0 : ao + A_SLOT;
      step(P0{}, ao, bo1);
      step(P1{}, ao1, bo2);
      bo = bo2; ao = ao1;
    }
    asm volatile("s_nop 15\n\ts_nop 15" ::: "memory");
    asm volatile("" :: "v"(code_pf));
    STAMP(6);
    if (mh == 0 && c == 0) {
#pragma unroll
      for (int n = 0; n < 4; ++n) *LDSP(f32x4, LDS_CS + (ntile[n] * 16 + 4 * q) * 4) = acc[0][n];
    }
  }
  wait_lgkm0();
  __builtin_amdgcn_s_barrier();

  f32x4 cs[4];
#pragma unroll
  for (int n = 0; n < 4; ++n) cs[n] = *LDSP(f32x4, LDS_CS + (ntile[n] * 16 + 4 * q) * 4);
  float rstd[6], nmr[6];
#pragma unroll
  for (int m = 0; m < 6; ++m) {
    const f32x2 v = *LDSP(f32x2, LDS_ST + ((mbase + m) * 16 + c) * 8);
    rstd[m] = v[0]; nmr[m] = v[1];
  }

  char* a2 = ws + WS_A2;
  STAMP(7);
  if (!is_att) {
    const int ks = 20 + 4 * cbk + wq;
    constexpr float NL2E = -1.4426950408889634f;
    if (wave < 8) {
#pragma unroll
      for (int m = 0; m < 6; ++m) {
        const int trow = (mbase + m) * 16 + c;
        const int tok = tok0 + trow;
        const float rg = rstd[m] * NL2E, ng = nmr[m] * NL2E;
        const f32x2 rg2 = {rg, rg}, ng2 = {ng, ng}, rs2 = {rstd[m], rstd[m]}, nm2 = {nmr[m], nmr[m]};
        unsigned pkv[4];
#pragma unroll
        for (int n = 0; n < 2; ++n)
#pragma unroll
          for (int h = 0; h < 2; ++h) {
            const f32x2 ag = {acc[m][n][2 * h], acc[m][n][2 * h + 1]}, au = {acc[m][n + 2][2 * h], acc[m][n + 2][2 * h + 1]};
            const f32x2 cg = {cs[n][2 * h], cs[n][2 * h + 1]}, cu = {cs[n + 2][2 * h], cs[n + 2][2 * h + 1]};
            const f32x2 eg = __builtin_elementwise_fma(ag, rg2, ng2 * cg);
            const f32x2 uu = __builtin_elementwise_fma(au, rs2, nm2 * cu);
            f32x2 d = {__builtin_amdgcn_exp2f(eg[0]), __builtin_amdgcn_exp2f(eg[1])};
            d = d + f32x2{1.0f, 1.0f};
            const f32x2 hv = uu * f32x2{__builtin_amdgcn_rcpf(d[0]), __builtin_amdgcn_rcpf(d[1])};
            pkv[2 * n + h] = pack2(hv[0], hv[1]);
          }
        if (trow >= 1 && tok < S_)
          st_wt((u32x4*)(a2 + (size_t)(((tok >> 6) * NKS2 + ks) * 4 + ((tok >> 4) & 3)) * 1024 + (16 * q + (tok & 15)) * 16),
                u32x4{pkv[0], pkv[1], pkv[2], pkv[3]});
      }
    } else {
      retile_w2(ws, job, wq, 4, lane);
    }
  } else {
    if (wave < 8) {
#pragma unroll
      for (int m = 0; m < 5; ++m) {
        const int trow = (mbase + m) * 16 + c;
        const f32x2 rs2 = {rstd[m], rstd[m]}, nm2 = {nmr[m], nmr[m]};
#pragma unroll
        for (int n = 0; n < 4; ++n) {
          const f32x2 y0 = __builtin_elementwise_fma(f32x2{acc[m][n][0], acc[m][n][1]}, rs2, nm2 * f32x2{cs[n][0], cs[n][1]});
          const f32x2 y1 = __builtin_elementwise_fma(f32x2{acc[m][n][2], acc[m][n][3]}, rs2, nm2 * f32x2{cs[n][2], cs[n][3]});
          if (n < 3 || wq < 3)
            *LDSP(u32x2, trow * QROW + (ntile[n] * 16 + 4 * q) * 2) = u32x2{pack2(y0[0], y0[1]), pack2(y1[0], y1[1])};
        }
      }
    }
    float bias[8];
#pragma unroll
    for (int j = 0; j < 8; ++j) {
      const int diff = 16 + c - (16 * (j >> 2) + 4 * q + (j & 3));
      bias[j] = (diff >= 0 && diff <= 14 && (diff & 1) == 0) ? 0.f : -3.0e38f;
    }
    const float sl2 = 0.22360679774997896f * 1.4426950408889634f;
    const int dofs = q < 2 ? 16 * q : 32;
    const unsigned qk_l = c * QROW + dofs;
    const unsigned q0_l = q == 3 ? LDS_ZERO : qk_l + 16 * QROW, q0_m = q == 3 ? 0u : 1u;
    const unsigned q1_l = q >= 2 ? LDS_ZERO : qk_l + 16 * QROW + 8, q1_m = q >= 2 ? 0u : 1u;
    const unsigned v_l = (4 * q + (c >> 2)) * QROW + 320 + 8 * (c & 3);
    if (tid == 512) *LDSP(u32x2, LDS_ZERO) = u32x2{0u, 0u};
    wait_lgkm0();
    __builtin_amdgcn_s_barrier();
    STAMP(8);
#pragma unroll 1
    for (int unit = wave; unit < 36; unit += 12) {
      const int mq = 1 + (unit >> 2), hh = unit & 3;
      const unsigned ub = 16 * (mq - 1) * QROW + 40 * hh;
      const u32x2 q0 = *LDSP(u32x2, q0_l + ub * q0_m), q1 = *LDSP(u32x2, q1_l + ub * q1_m);
      const u32x2 k00 = *LDSP(u32x2, ub + qk_l + 160), k01 = *LDSP(u32x2, ub + qk_l + 168);
      const u32x2 k10 = *LDSP(u32x2, ub + qk_l + 16 * QROW + 160), k11 = *LDSP(u32x2, ub + qk_l + 16 * QROW + 168);
      const bf16x8 vf0 = tr_frag(ub + v_l, ub + v_l + 16 * QROW);
      const bf16x8 vf1 = tr_frag(ub + v_l + 32, ub + v_l + 32 + 16 * QROW);
      const bf16x8 qf = __builtin_bit_cast(bf16x8, u32x4{q0[0], q0[1], q1[0], q1[1]});
      const f32x4 z4 = {0.f, 0.f, 0.f, 0.f};
      const f32x4 s0 = __builtin_amdgcn_mfma_f32_16x16x32_bf16(__builtin_bit_cast(bf16x8, u32x4{k00[0], k00[1], k01[0], k01[1]}), qf, z4, 0, 0, 0);
      const f32x4 s1 = __builtin_amdgcn_mfma_f32_16x16x32_bf16(__builtin_bit_cast(bf16x8, u32x4{k10[0], k10[1], k11[0], k11[1]}), qf, z4, 0, 0, 0);
      float lg[8];
      const float sl0 = (tok0 + 16 * (mq - 1) < 0) ? 0.f : sl2;
      const float b0 = (tok0 + 16 * (mq - 1) < 0) ? -3.0e38f : 0.f;
#pragma unroll
      for (int j = 0; j < 4; ++j) {
        lg[j] = __builtin_fmaf(s0[j], sl0, fminf(bias[j], b0));
        lg[4 + j] = __builtin_fmaf(s1[j], sl2, bias[4 + j]);
      }
      float mx = fmaxf(fmaxf(fmaxf(lg[0], lg[1]), fmaxf(lg[2], lg[3])), fmaxf(fmaxf(lg[4], lg[5]), fmaxf(lg[6], lg[7])));
      mx = red_max4(mx);
      float sum = 0.f;
#pragma unroll
      for (int j = 0; j < 8; ++j) { lg[j] = __builtin_amdgcn_exp2f(lg[j] - mx); sum += lg[j]; }
      const float inv = __builtin_amdgcn_rcpf(red_sum4(sum));
      const f32x2 inv2 = {inv, inv};
      const f32x2 p0 = f32x2{lg[0], lg[1]} * inv2, p1 = f32x2{lg[2], lg[3]} * inv2;
      const f32x2 p2 = f32x2{lg[4], lg[5]} * inv2, p3 = f32x2{lg[6], lg[7]} * inv2;
      const bf16x8 pf = __builtin_bit_cast(bf16x8, u32x4{pack2(p0[0], p0[1]), pack2(p1[0], p1[1]), pack2(p2[0], p2[1]), pack2(p3[0], p3[1])});
      const f32x4 o0 = __builtin_amdgcn_mfma_f32_16x16x32_bf16(vf0, pf, z4, 0, 0, 0);
      const f32x4 o1 = __builtin_amdgcn_mfma_f32_16x16x32_bf16(vf1, pf, z4, 0, 0, 0);
      const int tokb = tok0 + 16 * mq;
      if (tokb < S_) {
        char* ob = a2 + (size_t)((tokb >> 6) * NKS2 * 4 + ((tokb >> 4) & 3)) * 1024 + c * 16;
        const int col0 = 80 * hg + 20 * hh + 4 * q;
        *(u32x2*)(ob + (col0 >> 5) * 4096 + ((col0 >> 2) & 3) * 256 + ((col0 >> 4) & 1) * 8) = u32x2{pack2(o0[0], o0[1]), pack2(o0[2], o0[3])};
        const int col1 = 80 * hg + 20 * hh + 16;
        if (q == 0)
          *(u32x2*)(ob + (col1 >> 5) * 4096 + ((col1 >> 2) & 3) * 256 + ((col1 >> 4) & 1) * 8) = u32x2{pack2(o1[0], o1[1]), pack2(o1[2], o1[3])};
      }
    }
  }
  STAMP(9);
  STAMP(10);
  STAMP_OUT(0);
}

constexpr int K2_DEPTH = 6;
__global__ __launch_bounds__(256, 1) void k2_kernel(const __hip_bfloat16* __restrict__ x,
                                                    char* __restrict__ ws,
                                                    __hip_bfloat16* __restrict__ out) {
  const int tid = threadIdx.x, lane = tid & 63;
  const int wave = __builtin_amdgcn_readfirstlane(tid >> 6);
  const int q = lane >> 4, c = lane & 15;
  const int xcd = blockIdx.x & 7, idx = blockIdx.x >> 3;
  const int rb = (xcd >> 1) * 8 + (idx >> 2), cb = (xcd & 1) * 4 + (idx & 3);
  STAMP_DECL;
  STAMP(0);

  const char* ap = ws + WS_A2 + (size_t)(rb * NKS2 + 17 * wave) * 4096 + lane * 16;
  const char* wp = ws + WS_W2 + (size_t)(cb * NKS2 + 17 * wave) * 5120 + lane * 16;

  bf16x8 fa[K2_DEPTH][4], fw[K2_DEPTH][5];
  f32x4 acc[4][5];
#pragma unroll
  for (int m = 0; m < 4; ++m)
#pragma unroll
    for (int n = 0; n < 5; ++n) acc[m][n] = f32x4{0.f, 0.f, 0.f, 0.f};

  auto ld = [&](int i) {
    const int s = i % K2_DEPTH;
#pragma unroll
    for (int m = 0; m < 4; ++m) fa[s][m] = *(const bf16x8*)(ap + i * 4096 + m * 1024);
#pragma unroll
    for (int n = 0; n < 5; ++n) fw[s][n] = *(const bf16x8*)(wp + i * 5120 + n * 1024);
  };
#pragma unroll
  for (int i = 0; i < K2_DEPTH; ++i) ld(i);
  const int tok = rb * 64 + wave * 16 + c;
  const size_t obase = (size_t)tok * D_ + cb * 80;
  const u32x4 x0 = *(const u32x4*)(x + obase + 8 * q);
  const u32x4 x1 = *(const u32x4*)(x + obase + 32 + 8 * q);
  const u32x2 x2 = *(const u32x2*)(x + obase + 64 + 4 * q);
  __builtin_amdgcn_sched_barrier(0);
#pragma unroll
  for (int i = 0; i < 17; ++i) {
    const int s = i % K2_DEPTH;
#pragma unroll
    for (int m = 0; m < 4; ++m)
#pragma unroll
      for (int n = 0; n < 5; ++n)
        acc[m][n] = __builtin_amdgcn_mfma_f32_16x16x32_bf16(fw[s][n], fa[s][m], acc[m][n], 0, 0, 0);
    __builtin_amdgcn_sched_barrier(0);
    if (i + K2_DEPTH < 17) ld(i + K2_DEPTH);
    __builtin_amdgcn_sched_barrier(0);
  }

#pragma unroll
  for (int m = 0; m < 4; ++m) {
    if (m != wave) {
      const int si = wave < m ? wave : wave - 1;
#pragma unroll
      for (int n = 0; n < 5; ++n) *LDSP(f32x4, ((m * 3 + si) * 5 + n) * 1024 + lane * 16) = acc[m][n];
    }
  }
  __syncthreads();
  f32x4 r[5];
#pragma unroll
  for (int n = 0; n < 5; ++n) {
    r[n] = wave == 0 ? acc[0][n] : (wave == 1 ? acc[1][n] : (wave == 2 ? acc[2][n] : acc[3][n]));
#pragma unroll
    for (int si = 0; si < 3; ++si) r[n] += *LDSP(f32x4, ((wave * 3 + si) * 5 + n) * 1024 + lane * 16);
  }
  auto lo = [](unsigned v) { return __builtin_bit_cast(float, v << 16); };
  auto hi = [](unsigned v) { return __builtin_bit_cast(float, v & 0xFFFF0000u); };
  u32x4 o0 = {pack2(r[0][0] + lo(x0[0]), r[0][1] + hi(x0[0])), pack2(r[0][2] + lo(x0[1]), r[0][3] + hi(x0[1])),
              pack2(r[1][0] + lo(x0[2]), r[1][1] + hi(x0[2])), pack2(r[1][2] + lo(x0[3]), r[1][3] + hi(x0[3]))};
  u32x4 o1 = {pack2(r[2][0] + lo(x1[0]), r[2][1] + hi(x1[0])), pack2(r[2][2] + lo(x1[1]), r[2][3] + hi(x1[1])),
              pack2(r[3][0] + lo(x1[2]), r[3][1] + hi(x1[2])), pack2(r[3][2] + lo(x1[3]), r[3][3] + hi(x1[3]))};
  u32x2 o2 = {pack2(r[4][0] + lo(x2[0]), r[4][1] + hi(x2[0])), pack2(r[4][2] + lo(x2[1]), r[4][3] + hi(x2[1]))};
  *(u32x4*)(out + obase + 8 * q) = o0;
  *(u32x4*)(out + obase + 32 + 8 * q) = o1;
  *(u32x2*)(out + obase + 64 + 4 * q) = o2;
  STAMP(6);
  STAMP_OUT(1);
}

extern "C" void kernel_launch(void* const* d_in, const int* in_sizes, int n_in,
                              void* d_out, int out_size, void* d_ws, size_t ws_size,
                              hipStream_t stream) {
  const __hip_bfloat16* x = (const __hip_bfloat16*)d_in[0];
  const __hip_bfloat16* wqkv = (const __hip_bfloat16*)d_in[1];
  const __hip_bfloat16* wo = (const __hip_bfloat16*)d_in[2];
  const __hip_bfloat16* wg = (const __hip_bfloat16*)d_in[4];
  const __hip_bfloat16* wu = (const __hip_bfloat16*)d_in[5];
  const __hip_bfloat16* wd = (const __hip_bfloat16*)d_in[6];
  (void)hipFuncSetAttribute((const void*)k1_kernel, hipFuncAttributeMaxDynamicSharedMemorySize, LDS_K1);
  k1_kernel<<<256, 768, LDS_K1, stream>>>(x, wqkv, wo, wg, wu, wd, (char*)d_ws);
  k2_kernel<<<256, 256, 61440, stream>>>(x, (char*)d_ws, (__hip_bfloat16*)d_out);
}
```

```cpp
#include <hip/hip_runtime.h>
#include <hip/hip_bf16.h>
#include <type_traits>

typedef short s16x4 __attribute__((ext_vector_type(4)));
typedef short s16x8 __attribute__((ext_vector_type(8)));
typedef __bf16 bf16x2 __attribute__((ext_vector_type(2)));
typedef __bf16 bf16x8 __attribute__((ext_vector_type(8)));
typedef float f32x2 __attribute__((ext_vector_type(2)));
typedef float f32x4 __attribute__((ext_vector_type(4)));
typedef unsigned u32x2 __attribute__((ext_vector_type(2)));
typedef unsigned u32x4 __attribute__((ext_vector_type(4)));
#define LDSP(T, a) ((__attribute__((address_space(3))) T*)(size_t)(unsigned)(a))

constexpr int S_ = 2048, D_ = 640, F_ = 1536;
constexpr int NKS2 = 68;
constexpr size_t WS_A2 = 0;
constexpr size_t WS_W2 = 9437184;

constexpr int A_SLOT = 24576;
constexpr int LDS_A = 0;
constexpr int B_SLOT = 16384;
constexpr int LDS_B = 3 * A_SLOT;
constexpr int LDS_ST = LDS_B + 5 * B_SLOT;
constexpr int LDS_CS = LDS_ST + 192 * 8;
constexpr int LDS_ZERO = LDS_CS + 1024;
constexpr int LDS_K1 = LDS_ZERO + 16;
constexpr int QROW = 488;

__device__ __forceinline__ void dma16(const void* sbase, unsigned voff, unsigned lds_addr) {
  unsigned keep;
  asm volatile("s_mov_b32 %0, m0\n\ts_mov_b32 m0, %3\n\ts_nop 0\n\tglobal_load_lds_dwordx4 %1, %2\n\ts_mov_b32 m0, %0"
               : "=&s"(keep) : "v"(voff), "s"(sbase), "s"(lds_addr) : "memory");
}
template <int N> __device__ __forceinline__ void wait_vm() { asm volatile("s_waitcnt vmcnt(%0)" ::"i"(N) : "memory"); }
__device__ __forceinline__ void wait_lgkm0() {
  __builtin_amdgcn_s_waitcnt(0xC07F);
  asm volatile("" ::: "memory");
}

__device__ __forceinline__ bf16x8 tr_frag(unsigned a0, unsigned a1) {
  s16x4 lo = __builtin_amdgcn_ds_read_tr16_b64_v4i16(LDSP(s16x4, a0));
  s16x4 hi = __builtin_amdgcn_ds_read_tr16_b64_v4i16(LDSP(s16x4, a1));
  s16x8 v = __builtin_shufflevector(lo, hi, 0, 1, 2, 3, 4, 5, 6, 7);
  return __builtin_bit_cast(bf16x8, v);
}
__device__ __forceinline__ void mfma_acc(f32x4& c, const bf16x8& a, const bf16x8& b) {
  asm volatile("v_mfma_f32_16x16x32_bf16 %0, %1, %2, %0" : "+a"(c) : "v"(a), "v"(b));
}
__device__ __forceinline__ void mfma_accv(f32x4& c, const bf16x8& a, const bf16x8& b) {
  asm volatile("v_mfma_f32_16x16x32_bf16 %0, %1, %2, %0" : "+v"(c) : "v"(a), "v"(b));
}
__device__ __forceinline__ void st_wt(u32x4* p, u32x4 v) {
  asm volatile("global_store_dwordx4 %0, %1, off sc0 sc1\n\ts_nop 1" :: "v"(p), "v"(v) : "memory");
}
__device__ __forceinline__ void swap16(float& a, float& b) {
  asm volatile("s_nop 1\n\tv_permlane16_swap_b32 %0, %1" : "+v"(a), "+v"(b));
}
__device__ __forceinline__ void swap32(float& a, float& b) {
  asm volatile("s_nop 1\n\tv_permlane32_swap_b32 %0, %1" : "+v"(a), "+v"(b));
}
__device__ __forceinline__ float red_max4(float a) {
  float b = a;
  swap16(a, b); a = fmaxf(a, b); b = a;
  swap32(a, b); return fmaxf(a, b);
}
__device__ __forceinline__ float red_sum4(float a) {
  float b = a;
  swap16(a, b); a = a + b; b = a;
  swap32(a, b); return a + b;
}
__device__ __forceinline__ unsigned pack2(float a, float b) {
  bf16x2 p = {(__bf16)a, (__bf16)b};
  return __builtin_bit_cast(unsigned, p);
}

#define STAMP(i)
#define STAMP_DECL
#define STAMP_OUT(k)
template <int I, int N, class Fn> __device__ __forceinline__ void static_for(Fn&& f) {
  if constexpr (I < N) { f(std::integral_constant<int, I>{}); static_for<I + 1, N>(f); }
}

#define SB() __builtin_amdgcn_sched_barrier(0)
#define PC_BACK 0

__device__ __forceinline__ void retile_w2(char* ws, int job, int u0, int ustep, int lane) {
  const int q = lane >> 4, c = lane & 15;
  const int ks2 = job >> 1, half = job & 1;
  const int a = c >> 2, p = c & 3;
#pragma unroll 1
  for (int u = u0; u < 20; u += ustep) {
    const int cl = u / 5, n5 = u - 5 * cl;
    const int colst = n5 < 4 ? 32 * (n5 >> 1) + 8 * p + 4 * (n5 & 1) : 64 + 4 * p;
    const unsigned ad = LDS_A + 1 * A_SLOT + (4 * q + a) * 640 + (80 * cl + colst) * 2;
    bf16x8 f = tr_frag(ad, ad + 16 * 640);
    const int cb2 = 4 * half + cl;
    st_wt((u32x4*)(ws + WS_W2 + (size_t)((cb2 * NKS2 + ks2) * 5 + n5) * 1024 + lane * 16), __builtin_bit_cast(u32x4, f));
  }
}

__global__ __launch_bounds__(768, 3) void k1_kernel(const __hip_bfloat16* __restrict__ x,
                                                    const __hip_bfloat16* __restrict__ wqkv,
                                                    const __hip_bfloat16* __restrict__ wo,
                                                    const __hip_bfloat16* __restrict__ wg,
                                                    const __hip_bfloat16* __restrict__ wu,
                                                    const __hip_bfloat16* __restrict__ wd,
                                                    char* __restrict__ ws) {
  STAMP_DECL;
  STAMP(0);
  const int tid = threadIdx.x, lane = tid & 63;
  const int wave = __builtin_amdgcn_readfirstlane(tid >> 6);
  const int mh = (wave >> 2) & 1, wq = wave & 3;
  const int q = lane >> 4, c = lane & 15;

  const int xcd = blockIdx.x & 7, idx = blockIdx.x >> 3;
  const int xr = xcd >> 2, xc = xcd & 3;
  const int natt = xr == 0 ? 14 : 16;
  const bool is_att = idx < natt;
  int tok0, cbk = 0, hg = 0, job = 0;
  if (is_att) {
    const int ab = (xr == 0 ? 0 : 7) + (idx >> 1);
    hg = 2 * xc + (idx & 1);
    tok0 = 144 * ab - 16;
  } else {
    const int j = idx - natt;
    if (xr == 1 && j >= 15) {
      const int jb = 132 + xc;
      const char* wb = (const char*)wd + (size_t)((jb >> 1) - 20) * 32 * 1280 + 640 * (jb & 1);
      for (int pc = wave; pc < 20; pc += 12) {
        const int o = 1024 * pc + 16 * lane;
        const int row = o / 640;
        dma16(wb, (unsigned)(row * 1280 + (o - row * 640)), LDS_A + A_SLOT + pc * 1024);
      }
      wait_vm<0>();
      __builtin_amdgcn_s_barrier();
      retile_w2(ws, jb, wave, 12, lane);
      return;
    }
    const int rb = 6 * xr + j / 3;
    cbk = 3 * xc + j % 3;
    tok0 = 191 * rb - 1;
    job = rb * 12 + cbk;
  }
  const int mbase = is_att ? 5 * mh : 6 * mh;
  int ntile[4];
#pragma unroll
  for (int n = 0; n < 4; ++n) ntile[n] = is_att ? 4 * wq + n : (n < 2 ? 2 * wq + n : 8 + 2 * wq + n - 2);
  f32x4 acc[6][4];

  if (wave >= 8) {
    const int L = wq;
    unsigned b_voff[4];
    const char* b_base[2];
    int b_step;
#pragma unroll
    for (int j = 0; j < 4; ++j) {
      const int row = 4 * (L + 4 * (j & 1)) + (lane >> 4);
      const int hsw = (row & 3) | (((row >> 3) & 1) << 2);
      const int g = (lane & 15) ^ (hsw << 1);
      if (is_att) {
        int Lc = 16 * (j >> 1) + g;
        Lc = Lc > 29 ? 29 : Lc;
        const int sec = Lc / 10;
        b_voff[j] = (unsigned)(row * 3840 + (640 * sec + 80 * hg + 8 * (Lc - 10 * sec)) * 2);
      } else {
        b_voff[j] = (unsigned)(row * 3072 + (128 * cbk + 8 * g) * 2);
      }
    }
    if (is_att) { b_base[0] = (const char*)wqkv; b_base[1] = (const char*)wqkv; b_step = 32 * 3840; }
    else { b_base[0] = (const char*)wg; b_base[1] = (const char*)wu; b_step = 32 * 3072; }
    auto issueB = [&](int j, unsigned slot_off) {
#pragma unroll
      for (int e = 0; e < 4; ++e)
        dma16(b_base[e >> 1] + (size_t)j * b_step, b_voff[e],
              LDS_B + slot_off + (L + 4 * e) * 1024);
    };
    unsigned a_voff[6];
#pragma unroll
    for (int e = 0; e < 6; ++e) {
      const int ii = (e < 5 || !is_att) ? L + 4 * e : L + 16;
      const int row = 8 * ii + (lane >> 3);
      int tok = tok0 + row;
      tok = tok < 0 ? 0 : (tok > S_ - 1 ? S_ - 1 : tok);
      a_voff[e] = (unsigned)(tok * 1280 + (((lane & 7) ^ ((row >> 1) & 7)) << 4));
    }
    const unsigned a_dst5 = !is_att ? 20480u : 16384u;
    const bool a_on0 = L != 0 || lane >= 8;
    auto issueA = [&](int j, int h, unsigned slot_off) {
      const char* ab = (const char*)x + j * 128;
      const unsigned dst = LDS_A + slot_off + L * 1024;
      if (h == 0) {
        if (a_on0) dma16(ab, a_voff[0], dst);
        dma16(ab, a_voff[1], dst + 4096);
        dma16(ab, a_voff[2], dst + 8192);
      } else {
        dma16(ab, a_voff[3], dst + 12288);
        dma16(ab, a_voff[4], dst + 16384);
        dma16(ab, a_voff[5], dst + a_dst5);
      }
    };
    issueA(0, 0, 0); issueB(0, 0); issueA(0, 1, 0); issueB(1, B_SLOT); issueA(1, 0, A_SLOT); issueB(2, 2 * B_SLOT);
    if (tid - 512 < 24) {
      u32x4 ones = {0x3F803F80u, 0x3F803F80u, 0x3F803F80u, 0x3F803F80u};
      *LDSP(u32x4, LDS_A + ((tid - 512) >> 3) * A_SLOT + (tid & 7) * 16) = ones;
    }
    f32x4 gq[3], gs[3];
#pragma unroll
    for (int i = 0; i < 3; ++i) { gq[i] = f32x4{0.f, 0.f, 0.f, 0.f}; gs[i] = f32x4{0.f, 0.f, 0.f, 0.f}; }
    const bf16x8 ones8 = __builtin_bit_cast(bf16x8, u32x4{0x3F803F80u, 0x3F803F80u, 0x3F803F80u, 0x3F803F80u});
    const unsigned s_rd = LDS_A + L * 2048 + c * 128;
    bf16x8 sf[2][3];
    auto st_rd = [&](int sub, unsigned ao) {
      const unsigned ad = s_rd + ao + (((4 * sub + q) ^ ((c >> 1) & 7)) << 4);
#pragma unroll
      for (int i = 0; i < 3; ++i) sf[sub][i] = *LDSP(bf16x8, ad + i * 8192);
    };
    auto st_mma = [&](int sub) {
#pragma unroll
      for (int i = 0; i < 3; ++i) {
        gq[i] = __builtin_amdgcn_mfma_f32_16x16x32_bf16(sf[sub][i], sf[sub][i], gq[i], 0, 0, 0);
        gs[i] = __builtin_amdgcn_mfma_f32_16x16x32_bf16(ones8, sf[sub][i], gs[i], 0, 0, 0);
      }
    };
    auto issueJob = [&](int h) {
      const int ks = job >> 1, half = job & 1;
      const char* wb = ks < 20 ? (const char*)wo + (size_t)ks * 32 * 1280 : (const char*)wd + (size_t)(ks - 20) * 32 * 1280;
#pragma unroll
      for (int e = 3 * h; e < 3 * h + 3; ++e) {
        const int pe = e < 5 ? e : 4;
        const int o = 1024 * (L + 4 * pe) + 16 * lane;
        const int row = o / 640;
        dma16(wb, (unsigned)(row * 1280 + 640 * half + (o - row * 640)), LDS_A + A_SLOT + (L + 4 * pe) * 1024);
      }
    };
    wait_vm<11>();
    wait_lgkm0();
    __builtin_amdgcn_s_barrier();
    st_rd(0, 0);
    issueA(1, 1, A_SLOT); issueB(3, 3 * B_SLOT);
    st_mma(0);
    unsigned bo = 0, ao = 0;
#pragma unroll 1
    for (int t = 0; t < 20; t += 2) {
      const unsigned bo1 = bo == 4 * B_SLOT ? 0 : bo + B_SLOT;
      const unsigned bo2 = bo1 == 4 * B_SLOT ? 0 : bo1 + B_SLOT;
      const unsigned ao1 = ao == 2 * A_SLOT ? 0 : ao + A_SLOT;
      const unsigned ao2 = ao1 == 2 * A_SLOT ? 0 : ao1 + A_SLOT;
      if (t == 0) wait_vm<14>(); else if (t <= 14) wait_vm<21>(); else if (t == 16) wait_vm<17>(); else wait_vm<6>();
      wait_lgkm0();
      __builtin_amdgcn_s_barrier();
      st_rd(1, ao);
      SB();
      if (t == 0) { issueA(2, 0, 2 * A_SLOT); issueB(4, 4 * B_SLOT); }
      if (t <= 14) { issueA((t >> 1) + 2, 1, ao2); issueB(t + 5, bo); }
      else if (t == 16) issueJob(1);
      SB();
      st_mma(1);
      SB();
      if (t <= 14) wait_vm<18>(); else if (t == 16) wait_vm<10>(); else wait_vm<0>();
      wait_lgkm0();
      __builtin_amdgcn_s_barrier();
      if (t < 18) {
        st_rd(0, ao1);
        SB();
        if (t <= 12) { issueA((t >> 1) + 3, 0, ao); issueB(t + 6, bo1); }
        else if (t == 14) issueJob(0);
        SB();
        st_mma(0);
        SB();
      }
      bo = bo2; ao = ao1;
    }
#pragma unroll
    for (int i = 0; i < 3; ++i) {
      const int r = c & 3;
      const float b = r == 0 ? gq[i][0] : (r == 1 ? gq[i][1] : (r == 2 ? gq[i][2] : gq[i][3]));
      if (q == (c >> 2)) {
        const float mu = gs[i][0] * (1.0f / D_);
        float var = b * (1.0f / D_) - mu * mu;
        var = var < 0.f ? 0.f : var;
        const float rs = __builtin_amdgcn_rsqf(var + 1e-5f);
        *LDSP(f32x2, LDS_ST + ((L + 4 * i) * 16 + c) * 8) = f32x2{rs, -mu * rs};
      }
    }
#pragma unroll
    for (int m = 0; m < 6; ++m)
#pragma unroll
      for (int n = 0; n < 4; ++n) acc[m][n] = f32x4{0.f, 0.f, 0.f, 0.f};
  } else {
    unsigned code_pf = 0;
    if (wave < 6) {
      const char* pc = (const char*)(__builtin_amdgcn_s_getpc() & ~255ull);
      code_pf = *(const unsigned*)(pc - PC_BACK + wave * 4096 + lane * 64);
    }
    unsigned a_rd[2];
#pragma unroll
    for (int s = 0; s < 2; ++s)
      a_rd[s] = LDS_A + mbase * 2048 + c * 128 + (((4 * s + q) ^ ((c >> 1) & 7)) << 4);
    unsigned b_rd[4];
    {
      const int a = c >> 2, p = c & 3;
      const int hh = a | ((q & 1) << 2);
#pragma unroll
      for (int n = 0; n < 4; ++n)
        b_rd[n] = LDS_B + (ntile[n] >> 3) * 8192 + (8 * q + a) * 256 + (((ntile[n] & 7) ^ hh) << 5) + 8 * p;
    }
#pragma unroll
    for (int m = 0; m < 6; ++m)
#pragma unroll
      for (int n = 0; n < 4; ++n) {
        acc[m][n] = f32x4{0.f, 0.f, 0.f, 0.f};
        if (m < 5) asm volatile("" : "+a"(acc[m][n])); else asm volatile("s_nop 1" : "+v"(acc[m][n]));
      }
    bf16x8 af[6], bfr[2][4];
    auto step = [&](auto par, unsigned aon, unsigned bon) {
      constexpr int P = decltype(par)::value;
      wait_lgkm0();
      __builtin_amdgcn_s_barrier();
      SB();
      static_for<0, 6>([&](auto mc) {
        constexpr int m = decltype(mc)::value;
        if (m < 5 || !is_att) {
#pragma unroll
          for (int n = 0; n < 4; ++n) {
            if constexpr (m < 5) mfma_acc(acc[m][n], bfr[P][n], af[m]);
            else mfma_accv(acc[m][n], bfr[P][n], af[m]);
          }
        }
        SB();
        af[m] = *LDSP(bf16x8, a_rd[1 - P] + aon + m * 2048);
        if constexpr (m < 4) bfr[1 - P][m] = tr_frag(b_rd[m] + bon, b_rd[m] + bon + 1024);
        SB();
      });
    };
    using P0 = std::integral_constant<int, 0>;
    using P1 = std::integral_constant<int, 1>;
    __builtin_amdgcn_s_barrier();
#pragma unroll
    for (int n = 0; n < 4; ++n) bfr[0][n] = tr_frag(b_rd[n], b_rd[n] + 1024);
#pragma unroll
    for (int m = 0; m < 6; ++m) af[m] = *LDSP(bf16x8, a_rd[0] + m * 2048);
    STAMP(1);
    unsigned bo = 0, ao = 0;
#pragma unroll 1
    for (int t = 0; t < 20; t += 2) {
      const unsigned bo1 = bo == 4 * B_SLOT ? 0 : bo + B_SLOT;
      const unsigned bo2 = bo1 == 4 * B_SLOT ? 0 : bo1 + B_SLOT;
      const unsigned ao1 = ao == 2 * A_SLOT ? 0 : ao + A_SLOT;
      step(P0{}, ao, bo1);
      step(P1{}, ao1, bo2);
      bo = bo2; ao = ao1;
    }
    asm volatile("s_nop 15\n\ts_nop 15" ::: "memory");
    asm volatile("" :: "v"(code_pf));
    STAMP(6);
    if (mh == 0 && c == 0) {
#pragma unroll
      for (int n = 0; n < 4; ++n) *LDSP(f32x4, LDS_CS + (ntile[n] * 16 + 4 * q) * 4) = acc[0][n];
    }
  }
  wait_lgkm0();
  __builtin_amdgcn_s_barrier();

  f32x4 cs[4];
#pragma unroll
  for (int n = 0; n < 4; ++n) cs[n] = *LDSP(f32x4, LDS_CS + (ntile[n] * 16 + 4 * q) * 4);
  float rstd[6], nmr[6];
#pragma unroll
  for (int m = 0; m < 6; ++m) {
    const f32x2 v = *LDSP(f32x2, LDS_ST + ((mbase + m) * 16 + c) * 8);
    rstd[m] = v[0]; nmr[m] = v[1];
  }

  char* a2 = ws + WS_A2;
  STAMP(7);
  if (!is_att) {
    const int ks = 20 + 4 * cbk + wq;
    constexpr float NL2E = -1.4426950408889634f;
    if (wave < 8) {
#pragma unroll
      for (int m = 0; m < 6; ++m) {
        const int trow = (mbase + m) * 16 + c;
        const int tok = tok0 + trow;
        const float rg = rstd[m] * NL2E, ng = nmr[m] * NL2E;
        const f32x2 rg2 = {rg, rg}, ng2 = {ng, ng}, rs2 = {rstd[m], rstd[m]}, nm2 = {nmr[m], nmr[m]};
        unsigned pkv[4];
#pragma unroll
        for (int n = 0; n < 2; ++n)
#pragma unroll
          for (int h = 0; h < 2; ++h) {
            const f32x2 ag = {acc[m][n][2 * h], acc[m][n][2 * h + 1]}, au = {acc[m][n + 2][2 * h], acc[m][n + 2][2 * h + 1]};
            const f32x2 cg = {cs[n][2 * h], cs[n][2 * h + 1]}, cu = {cs[n + 2][2 * h], cs[n + 2][2 * h + 1]};
            const f32x2 eg = __builtin_elementwise_fma(ag, rg2, ng2 * cg);
            const f32x2 uu = __builtin_elementwise_fma(au, rs2, nm2 * cu);
            f32x2 d = {__builtin_amdgcn_exp2f(eg[0]), __builtin_amdgcn_exp2f(eg[1])};
            d = d + f32x2{1.0f, 1.0f};
            const f32x2 hv = uu * f32x2{__builtin_amdgcn_rcpf(d[0]), __builtin_amdgcn_rcpf(d[1])};
            pkv[2 * n + h] = pack2(hv[0], hv[1]);
          }
        if (trow >= 1 && tok < S_)
          st_wt((u32x4*)(a2 + (size_t)(((tok >> 6) * NKS2 + ks) * 4 + ((tok >> 4) & 3)) * 1024 + (16 * q + (tok & 15)) * 16),
                u32x4{pkv[0], pkv[1], pkv[2], pkv[3]});
      }
    } else {
      retile_w2(ws, job, wq, 4, lane);
    }
  } else {
    if (wave < 8) {
#pragma unroll
      for (int m = 0; m < 5; ++m) {
        const int trow = (mbase + m) * 16 + c;
        const f32x2 rs2 = {rstd[m], rstd[m]}, nm2 = {nmr[m], nmr[m]};
#pragma unroll
        for (int n = 0; n < 4; ++n) {
          const f32x2 y0 = __builtin_elementwise_fma(f32x2{acc[m][n][0], acc[m][n][1]}, rs2, nm2 * f32x2{cs[n][0], cs[n][1]});
          const f32x2 y1 = __builtin_elementwise_fma(f32x2{acc[m][n][2], acc[m][n][3]}, rs2, nm2 * f32x2{cs[n][2], cs[n][3]});
          if (n < 3 || wq < 3)
            *LDSP(u32x2, trow * QROW + (ntile[n] * 16 + 4 * q) * 2) = u32x2{pack2(y0[0], y0[1]), pack2(y1[0], y1[1])};
        }
      }
    }
    float bias[8];
#pragma unroll
    for (int j = 0; j < 8; ++j) {
      const int diff = 16 + c - (16 * (j >> 2) + 4 * q + (j & 3));
      bias[j] = (diff >= 0 && diff <= 14 && (diff & 1) == 0) ? 0.f : -3.0e38f;
    }
    const float sl2 = 0.22360679774997896f * 1.4426950408889634f;
    const int dofs = q < 2 ? 16 * q : 32;
    const unsigned qk_l = c * QROW + dofs;
    const unsigned q0_l = q == 3 ? LDS_ZERO : qk_l + 16 * QROW, q0_m = q == 3 ? 0u : 1u;
    const unsigned q1_l = q >= 2 ? LDS_ZERO : qk_l + 16 * QROW + 8, q1_m = q >= 2 ? 0u : 1u;
    const unsigned v_l = (4 * q + (c >> 2)) * QROW + 320 + 8 * (c & 3);
    if (tid == 512) *LDSP(u32x2, LDS_ZERO) = u32x2{0u, 0u};
    wait_lgkm0();
    __builtin_amdgcn_s_barrier();
    STAMP(8);
#pragma unroll 1
    for (int unit = wave; unit < 36; unit += 12) {
      const int mq = 1 + (unit >> 2), hh = unit & 3;
      const unsigned ub = 16 * (mq - 1) * QROW + 40 * hh;
      const u32x2 q0 = *LDSP(u32x2, q0_l + ub * q0_m), q1 = *LDSP(u32x2, q1_l + ub * q1_m);
      const u32x2 k00 = *LDSP(u32x2, ub + qk_l + 160), k01 = *LDSP(u32x2, ub + qk_l + 168);
      const u32x2 k10 = *LDSP(u32x2, ub + qk_l + 16 * QROW + 160), k11 = *LDSP(u32x2, ub + qk_l + 16 * QROW + 168);
      const bf16x8 vf0 = tr_frag(ub + v_l, ub + v_l + 16 * QROW);
      const bf16x8 vf1 = tr_frag(ub + v_l + 32, ub + v_l + 32 + 16 * QROW);
      const bf16x8 qf = __builtin_bit_cast(bf16x8, u32x4{q0[0], q0[1], q1[0], q1[1]});
      const f32x4 z4 = {0.f, 0.f, 0.f, 0.f};
      const f32x4 s0 = __builtin_amdgcn_mfma_f32_16x16x32_bf16(__builtin_bit_cast(bf16x8, u32x4{k00[0], k00[1], k01[0], k01[1]}), qf, z4, 0, 0, 0);
      const f32x4 s1 = __builtin_amdgcn_mfma_f32_16x16x32_bf16(__builtin_bit_cast(bf16x8, u32x4{k10[0], k10[1], k11[0], k11[1]}), qf, z4, 0, 0, 0);
      float lg[8];
      const float sl0 = (tok0 + 16 * (mq - 1) < 0) ? 0.f : sl2;
      const float b0 = (tok0 + 16 * (mq - 1) < 0) ? -3.0e38f : 0.f;
#pragma unroll
      for (int j = 0; j < 4; ++j) {
        lg[j] = __builtin_fmaf(s0[j], sl0, fminf(bias[j], b0));
        lg[4 + j] = __builtin_fmaf(s1[j], sl2, bias[4 + j]);
      }
      float mx = fmaxf(fmaxf(fmaxf(lg[0], lg[1]), fmaxf(lg[2], lg[3])), fmaxf(fmaxf(lg[4], lg[5]), fmaxf(lg[6], lg[7])));
      mx = red_max4(mx);
      float sum = 0.f;
#pragma unroll
      for (int j = 0; j < 8; ++j) { lg[j] = __builtin_amdgcn_exp2f(lg[j] - mx); sum += lg[j]; }
      const float inv = __builtin_amdgcn_rcpf(red_sum4(sum));
      const f32x2 inv2 = {inv, inv};
      const f32x2 p0 = f32x2{lg[0], lg[1]} * inv2, p1 = f32x2{lg[2], lg[3]} * inv2;
      const f32x2 p2 = f32x2{lg[4], lg[5]} * inv2, p3 = f32x2{lg[6], lg[7]} * inv2;
      const bf16x8 pf = __builtin_bit_cast(bf16x8, u32x4{pack2(p0[0], p0[1]), pack2(p1[0], p1[1]), pack2(p2[0], p2[1]), pack2(p3[0], p3[1])});
      const f32x4 o0 = __builtin_amdgcn_mfma_f32_16x16x32_bf16(vf0, pf, z4, 0, 0, 0);
      const f32x4 o1 = __builtin_amdgcn_mfma_f32_16x16x32_bf16(vf1, pf, z4, 0, 0, 0);
      const int tokb = tok0 + 16 * mq;
      if (tokb < S_) {
        char* ob = a2 + (size_t)((tokb >> 6) * NKS2 * 4 + ((tokb >> 4) & 3)) * 1024 + c * 16;
        const int col0 = 80 * hg + 20 * hh + 4 * q;
        *(u32x2*)(ob + (col0 >> 5) * 4096 + ((col0 >> 2) & 3) * 256 + ((col0 >> 4) & 1) * 8) = u32x2{pack2(o0[0], o0[1]), pack2(o0[2], o0[3])};
        const int col1 = 80 * hg + 20 * hh + 16;
        if (q == 0)
          *(u32x2*)(ob + (col1 >> 5) * 4096 + ((col1 >> 2) & 3) * 256 + ((col1 >> 4) & 1) * 8) = u32x2{pack2(o1[0], o1[1]), pack2(o1[2], o1[3])};
      }
    }
  }
  STAMP(9);
  STAMP(10);
  STAMP_OUT(0);
}

constexpr int K2_DEPTH = 6;

__global__ __launch_bounds__(256, 1) void k2_kernel(const __hip_bfloat16* __restrict__ x,
                                                    char* __restrict__ ws,
                                                    __hip_bfloat16* __restrict__ out) {
  const int tid = threadIdx.x, lane = tid & 63;
  const int wave = __builtin_amdgcn_readfirstlane(tid >> 6);
  const int q = lane >> 4, c = lane & 15;
  const int xcd = blockIdx.x & 7, idx = blockIdx.x >> 3;
  const int rb = (xcd >> 1) * 8 + (idx >> 2), cb = (xcd & 1) * 4 + (idx & 3);
  STAMP_DECL;
  STAMP(0);

  const char* ap = ws + WS_A2 + (size_t)(rb * NKS2 + 17 * wave) * 4096 + lane * 16;
  const char* wp = ws + WS_W2 + (size_t)(cb * NKS2 + 17 * wave) * 5120 + lane * 16;

  bf16x8 fa[K2_DEPTH][4], fw[K2_DEPTH][5];
  f32x4 acc[4][5];
#pragma unroll
  for (int m = 0; m < 4; ++m)
#pragma unroll
    for (int n = 0; n < 5; ++n) acc[m][n] = f32x4{0.f, 0.f, 0.f, 0.f};

  auto ld = [&](int i) {
    const int s = i % K2_DEPTH;
    const int kk = i;
#pragma unroll
    for (int m = 0; m < 4; ++m) fa[s][m] = *(const bf16x8*)(ap + kk * 4096 + m * 1024);
#pragma unroll
    for (int n = 0; n < 5; ++n) fw[s][n] = *(const bf16x8*)(wp + kk * 5120 + n * 1024);
  };
#pragma unroll
  for (int i = 0; i < K2_DEPTH; ++i) ld(i);
  const int tok = rb * 64 + wave * 16 + c;
  const size_t obase = (size_t)tok * D_ + cb * 80;
  const u32x4 x0 = *(const u32x4*)(x + obase + 8 * q);
  const u32x4 x1 = *(const u32x4*)(x + obase + 32 + 8 * q);
  const u32x2 x2 = *(const u32x2*)(x + obase + 64 + 4 * q);
  __builtin_amdgcn_sched_barrier(0);
#pragma unroll
  for (int i = 0; i < 17; ++i) {
    const int s = i % K2_DEPTH;
#pragma unroll
    for (int m = 0; m < 4; ++m)
#pragma unroll
      for (int n = 0; n < 5; ++n)
        acc[m][n] = __builtin_amdgcn_mfma_f32_16x16x32_bf16(fw[s][n], fa[s][m], acc[m][n], 0, 0, 0);
    __builtin_amdgcn_sched_barrier(0);
    if (i + K2_DEPTH < 17) ld(i + K2_DEPTH);
    __builtin_amdgcn_sched_barrier(0);
  }

#pragma unroll
  for (int m = 0; m < 4; ++m) {
    if (m != wave) {
      const int si = wave < m ? wave : wave - 1;
#pragma unroll
      for (int n = 0; n < 5; ++n) *LDSP(f32x4, ((m * 3 + si) * 5 + n) * 1024 + lane * 16) = acc[m][n];
    }
  }
  __syncthreads();
  f32x4 r[5];
#pragma unroll
  for (int n = 0; n < 5; ++n) {
    r[n] = wave == 0 ? acc[0][n] : (wave == 1 ? acc[1][n] : (wave == 2 ? acc[2][n] : acc[3][n]));
#pragma unroll
    for (int si = 0; si < 3; ++si) r[n] += *LDSP(f32x4, ((wave * 3 + si) * 5 + n) * 1024 + lane * 16);
  }
  auto lo = [](unsigned v) { return __builtin_bit_cast(float, v << 16); };
  auto hi = [](unsigned v) { return __builtin_bit_cast(float, v & 0xFFFF0000u); };
  u32x4 o0 = {pack2(r[0][0] + lo(x0[0]), r[0][1] + hi(x0[0])), pack2(r[0][2] + lo(x0[1]), r[0][3] + hi(x0[1])),
              pack2(r[1][0] + lo(x0[2]), r[1][1] + hi(x0[2])), pack2(r[1][2] + lo(x0[3]), r[1][3] + hi(x0[3]))};
  u32x4 o1 = {pack2(r[2][0] + lo(x1[0]), r[2][1] + hi(x1[0])), pack2(r[2][2] + lo(x1[1]), r[2][3] + hi(x1[1])),
              pack2(r[3][0] + lo(x1[2]), r[3][1] + hi(x1[2])), pack2(r[3][2] + lo(x1[3]), r[3][3] + hi(x1[3]))};
  u32x2 o2 = {pack2(r[4][0] + lo(x2[0]), r[4][1] + hi(x2[0])), pack2(r[4][2] + lo(x2[1]), r[4][3] + hi(x2[1]))};
  *(u32x4*)(out + obase + 8 * q) = o0;
  *(u32x4*)(out + obase + 32 + 8 * q) = o1;
  *(u32x2*)(out + obase + 64 + 4 * q) = o2;
  STAMP(6);
  STAMP_OUT(1);
}

extern "C" void kernel_launch(void* const* d_in, const int* in_sizes, int n_in,
                              void* d_out, int out_size, void* d_ws, size_t ws_size,
                              hipStream_t stream) {
  const __hip_bfloat16* x = (const __hip_bfloat16*)d_in[0];
  const __hip_bfloat16* wqkv = (const __hip_bfloat16*)d_in[1];
  const __hip_bfloat16* wo = (const __hip_bfloat16*)d_in[2];
  const __hip_bfloat16* wg = (const __hip_bfloat16*)d_in[4];
  const __hip_bfloat16* wu = (const __hip_bfloat16*)d_in[5];
  const __hip_bfloat16* wd = (const __hip_bfloat16*)d_in[6];
  (void)hipFuncSetAttribute((const void*)k1_kernel, hipFuncAttributeMaxDynamicSharedMemorySize, LDS_K1);
  k1_kernel<<<256, 768, LDS_K1, stream>>>(x, wqkv, wo, wg, wu, wd, (char*)d_ws);
  k2_kernel<<<256, 256, 61440, stream>>>(x, (char*)d_ws, (__hip_bfloat16*)d_out);
}
```

```cpp
#include <hip/hip_runtime.h>
#include <hip/hip_bf16.h>
#include <type_traits>

typedef short s16x4 __attribute__((ext_vector_type(4)));
typedef short s16x8 __attribute__((ext_vector_type(8)));
typedef __bf16 bf16x2 __attribute__((ext_vector_type(2)));
typedef __bf16 bf16x8 __attribute__((ext_vector_type(8)));
typedef float f32x2 __attribute__((ext_vector_type(2)));
typedef float f32x4 __attribute__((ext_vector_type(4)));
typedef unsigned u32x2 __attribute__((ext_vector_type(2)));
typedef unsigned u32x4 __attribute__((ext_vector_type(4)));
#define LDSP(T, a) ((__attribute__((address_space(3))) T*)(size_t)(unsigned)(a))

constexpr int S_ = 2048, D_ = 640, F_ = 1536;
constexpr int NKS2 = 68;
constexpr size_t WS_A2 = 0;
constexpr size_t WS_W2 = 9437184;

constexpr int A_SLOT = 24576;
constexpr int LDS_A = 0;
constexpr int B_SLOT = 16384;
constexpr int LDS_B = 3 * A_SLOT;
constexpr int LDS_ST = LDS_B + 5 * B_SLOT;
constexpr int LDS_CS = LDS_ST + 192 * 8;
constexpr int LDS_ZERO = LDS_CS + 1024;
constexpr int LDS_K1 = LDS_ZERO + 16;
constexpr int QROW = 488;

__device__ __forceinline__ void dma16(const void* sbase, unsigned voff, unsigned lds_addr) {
  unsigned keep;
  asm volatile("s_mov_b32 %0, m0\n\ts_mov_b32 m0, %3\n\ts_nop 0\n\tglobal_load_lds_dwordx4 %1, %2\n\ts_mov_b32 m0, %0"
               : "=&s"(keep) : "v"(voff), "s"(sbase), "s"(lds_addr) : "memory");
}
template <int N> __device__ __forceinline__ void wait_vm() { asm volatile("s_waitcnt vmcnt(%0)" ::"i"(N) : "memory"); }
__device__ __forceinline__ void wait_lgkm0() {
  __builtin_amdgcn_s_waitcnt(0xC07F);
  asm volatile("" ::: "memory");
}

__device__ __forceinline__ bf16x8 tr_frag(unsigned a0, unsigned a1) {
  s16x4 lo = __builtin_amdgcn_ds_read_tr16_b64_v4i16(LDSP(s16x4, a0));
  s16x4 hi = __builtin_amdgcn_ds_read_tr16_b64_v4i16(LDSP(s16x4, a1));
  s16x8 v = __builtin_shufflevector(lo, hi, 0, 1, 2, 3, 4, 5, 6, 7);
  return __builtin_bit_cast(bf16x8, v);
}
__device__ __forceinline__ void mfma_acc(f32x4& c, const bf16x8& a, const bf16x8& b) {
  asm volatile("v_mfma_f32_16x16x32_bf16 %0, %1, %2, %0" : "+a"(c) : "v"(a), "v"(b));
}
__device__ __forceinline__ void mfma_accv(f32x4& c, const bf16x8& a, const bf16x8& b) {
  asm volatile("v_mfma_f32_16x16x32_bf16 %0, %1, %2, %0" : "+v"(c) : "v"(a), "v"(b));
}
__device__ __forceinline__ void st_wt(u32x4* p, u32x4 v) {
  asm volatile("global_store_dwordx4 %0, %1, off sc0 sc1\n\ts_nop 1" :: "v"(p), "v"(v) : "memory");
}
__device__ __forceinline__ void swap16(float& a, float& b) {
  asm volatile("s_nop 1\n\tv_permlane16_swap_b32 %0, %1" : "+v"(a), "+v"(b));
}
__device__ __forceinline__ void swap32(float& a, float& b) {
  asm volatile("s_nop 1\n\tv_permlane32_swap_b32 %0, %1" : "+v"(a), "+v"(b));
}
__device__ __forceinline__ float red_max4(float a) {
  float b = a;
  swap16(a, b); a = fmaxf(a, b); b = a;
  swap32(a, b); return fmaxf(a, b);
}
__device__ __forceinline__ float red_sum4(float a) {
  float b = a;
  swap16(a, b); a = a + b; b = a;
  swap32(a, b); return a + b;
}
__device__ __forceinline__ unsigned pack2(float a, float b) {
  bf16x2 p = {(__bf16)a, (__bf16)b};
  return __builtin_bit_cast(unsigned, p);
}

template <int I, int N, class Fn> __device__ __forceinline__ void static_for(Fn&& f) {
  if constexpr (I < N) { f(std::integral_constant<int, I>{}); static_for<I + 1, N>(f); }
}

#define SB() __builtin_amdgcn_sched_barrier(0)

__device__ __forceinline__ void retile_w2(char* ws, int job, int u0, int ustep, int lane) {
  const int q = lane >> 4, c = lane & 15;
  const int ks2 = job >> 1, half = job & 1;
  const int a = c >> 2, p = c & 3;
#pragma unroll 1
  for (int u = u0; u < 20; u += ustep) {
    const int cl = u / 5, n5 = u - 5 * cl;
    const int colst = n5 < 4 ? 32 * (n5 >> 1) + 8 * p + 4 * (n5 & 1) : 64 + 4 * p;
    const unsigned ad = LDS_A + 1 * A_SLOT + (4 * q + a) * 640 + (80 * cl + colst) * 2;
    bf16x8 f = tr_frag(ad, ad + 16 * 640);
    const int cb2 = 4 * half + cl;
    st_wt((u32x4*)(ws + WS_W2 + (size_t)((cb2 * NKS2 + ks2) * 5 + n5) * 1024 + lane * 16), __builtin_bit_cast(u32x4, f));
  }
}

__global__ __launch_bounds__(768, 3) void k1_kernel(const __hip_bfloat16* __restrict__ x,
                                                    const __hip_bfloat16* __restrict__ wqkv,
                                                    const __hip_bfloat16* __restrict__ wo,
                                                    const __hip_bfloat16* __restrict__ wg,
                                                    const __hip_bfloat16* __restrict__ wu,
                                                    const __hip_bfloat16* __restrict__ wd,
                                                    char* __restrict__ ws) {
  const int tid = threadIdx.x, lane = tid & 63;
  const int wave = __builtin_amdgcn_readfirstlane(tid >> 6);
  const int mh = (wave >> 2) & 1, wq = wave & 3;
  const int q = lane >> 4, c = lane & 15;

  const int xcd = blockIdx.x & 7, idx = blockIdx.x >> 3;
  const int xr = xcd >> 2, xc = xcd & 3;
  const int natt = xr == 0 ? 14 : 16;
  const bool is_att = idx < natt;
  int tok0, cbk = 0, hg = 0, job = 0;
  if (is_att) {
    const int ab = (xr == 0 ? 0 : 7) + (idx >> 1);
    hg = 2 * xc + (idx & 1);
    tok0 = 144 * ab - 16;
  } else {
    const int j = idx - natt;
    if (xr == 1 && j >= 15) {
      const int jb = 132 + xc;
      const char* wb = (const char*)wd + (size_t)((jb >> 1) - 20) * 32 * 1280 + 640 * (jb & 1);
      for (int pc = wave; pc < 20; pc += 12) {
        const int o = 1024 * pc + 16 * lane;
        const int row = o / 640;
        dma16(wb, (unsigned)(row * 1280 + (o - row * 640)), LDS_A + A_SLOT + pc * 1024);
      }
      wait_vm<0>();
      __builtin_amdgcn_s_barrier();
      retile_w2(ws, jb, wave, 12, lane);
      return;
    }
    const int rb = 6 * xr + j / 3;
    cbk = 3 * xc + j % 3;
    tok0 = 191 * rb - 1;
    job = rb * 12 + cbk;
  }
  const int mbase = is_att ? 5 * mh : 6 * mh;
  int ntile[4];
#pragma unroll
  for (int n = 0; n < 4; ++n) ntile[n] = is_att ? 4 * wq + n : (n < 2 ? 2 * wq + n : 8 + 2 * wq + n - 2);
  f32x4 acc[6][4];

  if (wave >= 8) {
    const int L = wq;
    unsigned b_voff[4];
    const char* b_base[2];
    int b_step;
#pragma unroll
    for (int j = 0; j < 4; ++j) {
      const int row = 4 * (L + 4 * (j & 1)) + (lane >> 4);
      const int hsw = (row & 3) | (((row >> 3) & 1) << 2);
      const int g = (lane & 15) ^ (hsw << 1);
      if (is_att) {
        int Lc = 16 * (j >> 1) + g;
        Lc = Lc > 29 ? 29 : Lc;
        const int sec = Lc / 10;
        b_voff[j] = (unsigned)(row * 3840 + (640 * sec + 80 * hg + 8 * (Lc - 10 * sec)) * 2);
      } else {
        b_voff[j] = (unsigned)(row * 3072 + (128 * cbk + 8 * g) * 2);
      }
    }
    if (is_att) { b_base[0] = (const char*)wqkv; b_base[1] = (const char*)wqkv; b_step = 32 * 3840; }
    else { b_base[0] = (const char*)wg; b_base[1] = (const char*)wu; b_step = 32 * 3072; }
    auto issueB = [&](int j, unsigned slot_off) {
#pragma unroll
      for (int e = 0; e < 4; ++e)
        dma16(b_base[e >> 1] + (size_t)j * b_step, b_voff[e],
              LDS_B + slot_off + (L + 4 * e) * 1024);
    };
    unsigned a_voff[6];
#pragma unroll
    for (int e = 0; e < 6; ++e) {
      const int ii = (e < 5 || !is_att) ? L + 4 * e : L + 16;
      const int row = 8 * ii + (lane >> 3);
      int tok = tok0 + row;
      tok = tok < 0 ? 0 : (tok > S_ - 1 ? S_ - 1 : tok);
      a_voff[e] = (unsigned)(tok * 1280 + (((lane & 7) ^ ((row >> 1) & 7)) << 4));
    }
    const unsigned a_dst5 = !is_att ? 20480u : 16384u;
    const bool a_on0 = L != 0 || lane >= 8;
    auto issueA = [&](int j, int h, unsigned slot_off) {
      const char* ab = (const char*)x + j * 128;
      const unsigned dst = LDS_A + slot_off + L * 1024;
      if (h == 0) {
        if (a_on0) dma16(ab, a_voff[0], dst);
        dma16(ab, a_voff[1], dst + 4096);
        dma16(ab, a_voff[2], dst + 8192);
      } else {
        dma16(ab, a_voff[3], dst + 12288);
        dma16(ab, a_voff[4], dst + 16384);
        dma16(ab, a_voff[5], dst + a_dst5);
      }
    };
    issueA(0, 0, 0); issueB(0, 0); issueA(0, 1, 0); issueB(1, B_SLOT); issueA(1, 0, A_SLOT); issueB(2, 2 * B_SLOT);
    if (tid - 512 < 24) {
      u32x4 ones = {0x3F803F80u, 0x3F803F80u, 0x3F803F80u, 0x3F803F80u};
      *LDSP(u32x4, LDS_A + ((tid - 512) >> 3) * A_SLOT + (tid & 7) * 16) = ones;
    }
    f32x4 gq[3], gs[3];
#pragma unroll
    for (int i = 0; i < 3; ++i) { gq[i] = f32x4{0.f, 0.f, 0.f, 0.f}; gs[i] = f32x4{0.f, 0.f, 0.f, 0.f}; }
    const bf16x8 ones8 = __builtin_bit_cast(bf16x8, u32x4{0x3F803F80u, 0x3F803F80u, 0x3F803F80u, 0x3F803F80u});
    const unsigned s_rd = LDS_A + L * 2048 + c * 128;
    bf16x8 sf[2][3];
    auto st_rd = [&](int sub, unsigned ao) {
      const unsigned ad = s_rd + ao + (((4 * sub + q) ^ ((c >> 1) & 7)) << 4);
#pragma unroll
      for (int i = 0; i < 3; ++i) sf[sub][i] = *LDSP(bf16x8, ad + i * 8192);
    };
    auto st_mma = [&](int sub) {
#pragma unroll
      for (int i = 0; i < 3; ++i) {
        gq[i] = __builtin_amdgcn_mfma_f32_16x16x32_bf16(sf[sub][i], sf[sub][i], gq[i], 0, 0, 0);
        gs[i] = __builtin_amdgcn_mfma_f32_16x16x32_bf16(ones8, sf[sub][i], gs[i], 0, 0, 0);
      }
    };
    auto issueJob = [&](int h) {
      const int ks = job >> 1, half = job & 1;
      const char* wb = ks < 20 ? (const char*)wo + (size_t)ks * 32 * 1280 : (const char*)wd + (size_t)(ks - 20) * 32 * 1280;
#pragma unroll
      for (int e = 3 * h; e < 3 * h + 3; ++e) {
        const int pe = e < 5 ? e : 4;
        const int o = 1024 * (L + 4 * pe) + 16 * lane;
        const int row = o / 640;
        dma16(wb, (unsigned)(row * 1280 + 640 * half + (o - row * 640)), LDS_A + A_SLOT + (L + 4 * pe) * 1024);
      }
    };
    wait_vm<11>();
    wait_lgkm0();
    __builtin_amdgcn_s_barrier();
    st_rd(0, 0);
    issueA(1, 1, A_SLOT); issueB(3, 3 * B_SLOT);
    st_mma(0);
    unsigned bo = 0, ao = 0;
#pragma unroll 1
    for (int t = 0; t < 20; t += 2) {
      const unsigned bo1 = bo == 4 * B_SLOT ? 0 : bo + B_SLOT;
      const unsigned bo2 = bo1 == 4 * B_SLOT ? 0 : bo1 + B_SLOT;
      const unsigned ao1 = ao == 2 * A_SLOT ? 0 : ao + A_SLOT;
      const unsigned ao2 = ao1 == 2 * A_SLOT ? 0 : ao1 + A_SLOT;
      if (t == 0) wait_vm<14>(); else if (t <= 14) wait_vm<21>(); else if (t == 16) wait_vm<17>(); else wait_vm<6>();
      wait_lgkm0();
      __builtin_amdgcn_s_barrier();
      st_rd(1, ao);
      SB();
      if (t == 0) { issueA(2, 0, 2 * A_SLOT); issueB(4, 4 * B_SLOT); }
      if (t <= 14) { issueA((t >> 1) + 2, 1, ao2); issueB(t + 5, bo); }
      else if (t == 16) issueJob(1);
      SB();
      st_mma(1);
      SB();
      if (t <= 14) wait_vm<18>(); else if (t == 16) wait_vm<10>(); else wait_vm<0>();
      wait_lgkm0();
      __builtin_amdgcn_s_barrier();
      if (t < 18) {
        st_rd(0, ao1);
        SB();
        if (t <= 12) { issueA((t >> 1) + 3, 0, ao); issueB(t + 6, bo1); }
        else if (t == 14) issueJob(0);
        SB();
        st_mma(0);
        SB();
      }
      bo = bo2; ao = ao1;
    }
#pragma unroll
    for (int i = 0; i < 3; ++i) {
      const int r = c & 3;
      const float b = r == 0 ? gq[i][0] : (r == 1 ? gq[i][1] : (r == 2 ? gq[i][2] : gq[i][3]));
      if (q == (c >> 2)) {
        const float mu = gs[i][0] * (1.0f / D_);
        float var = b * (1.0f / D_) - mu * mu;
        var = var < 0.f ? 0.f : var;
        const float rs = __builtin_amdgcn_rsqf(var + 1e-5f);
        *LDSP(f32x2, LDS_ST + ((L + 4 * i) * 16 + c) * 8) = f32x2{rs, -mu * rs};
      }
    }
#pragma unroll
    for (int m = 0; m < 6; ++m)
#pragma unroll
      for (int n = 0; n < 4; ++n) acc[m][n] = f32x4{0.f, 0.f, 0.f, 0.f};
  } else {
    unsigned a_rd[2];
#pragma unroll
    for (int s = 0; s < 2; ++s)
      a_rd[s] = LDS_A + mbase * 2048 + c * 128 + (((4 * s + q) ^ ((c >> 1) & 7)) << 4);
    unsigned b_rd[4];
    {
      const int a = c >> 2, p = c & 3;
      const int hh = a | ((q & 1) << 2);
#pragma unroll
      for (int n = 0; n < 4; ++n)
        b_rd[n] = LDS_B + (ntile[n] >> 3) * 8192 + (8 * q + a) * 256 + (((ntile[n] & 7) ^ hh) << 5) + 8 * p;
    }
#pragma unroll
    for (int m = 0; m < 6; ++m)
#pragma unroll
      for (int n = 0; n < 4; ++n) {
        acc[m][n] = f32x4{0.f, 0.f, 0.f, 0.f};
        if (m < 5) asm volatile("" : "+a"(acc[m][n])); else asm volatile("s_nop 1" : "+v"(acc[m][n]));
      }
    bf16x8 af[6], bfr[2][4];
    auto step = [&](auto par, unsigned aon, unsigned bon) {
      constexpr int P = decltype(par)::value;
      wait_lgkm0();
      __builtin_amdgcn_s_barrier();
      SB();
      static_for<0, 6>([&](auto mc) {
        constexpr int m = decltype(mc)::value;
        if (m < 5 || !is_att) {
#pragma unroll
          for (int n = 0; n < 4; ++n) {
            if constexpr (m < 5) mfma_acc(acc[m][n], bfr[P][n], af[m]);
            else mfma_accv(acc[m][n], bfr[P][n], af[m]);
          }
        }
        SB();
        af[m] = *LDSP(bf16x8, a_rd[1 - P] + aon + m * 2048);
        if constexpr (m < 4) bfr[1 - P][m] = tr_frag(b_rd[m] + bon, b_rd[m] + bon + 1024);
        SB();
      });
    };
    using P0 = std::integral_constant<int, 0>;
    using P1 = std::integral_constant<int, 1>;
    __builtin_amdgcn_s_barrier();
#pragma unroll
    for (int n = 0; n < 4; ++n) bfr[0][n] = tr_frag(b_rd[n], b_rd[n] + 1024);
#pragma unroll
    for (int m = 0; m < 6; ++m) af[m] = *LDSP(bf16x8, a_rd[0] + m * 2048);
    unsigned bo = 0, ao = 0;
#pragma unroll 1
    for (int t = 0; t < 20; t += 2) {
      const unsigned bo1 = bo == 4 * B_SLOT ? 0 : bo + B_SLOT;
      const unsigned bo2 = bo1 == 4 * B_SLOT ? 0 : bo1 + B_SLOT;
      const unsigned ao1 = ao == 2 * A_SLOT ? 0 : ao + A_SLOT;
      step(P0{}, ao, bo1);
      step(P1{}, ao1, bo2);
      bo = bo2; ao = ao1;
    }
    asm volatile("s_nop 15\n\ts_nop 15" ::: "memory");
    if (mh == 0 && c == 0) {
#pragma unroll
      for (int n = 0; n < 4; ++n) *LDSP(f32x4, LDS_CS + (ntile[n] * 16 + 4 * q) * 4) = acc[0][n];
    }
  }
  wait_lgkm0();
  __builtin_amdgcn_s_barrier();

  f32x4 cs[4];
#pragma unroll
  for (int n = 0; n < 4; ++n) cs[n] = *LDSP(f32x4, LDS_CS + (ntile[n] * 16 + 4 * q) * 4);
  float rstd[6], nmr[6];
#pragma unroll
  for (int m = 0; m < 6; ++m) {
    const f32x2 v = *LDSP(f32x2, LDS_ST + ((mbase + m) * 16 + c) * 8);
    rstd[m] = v[0]; nmr[m] = v[1];
  }

  char* a2 = ws + WS_A2;
  if (!is_att) {
    const int ks = 20 + 4 * cbk + wq;
    constexpr float NL2E = -1.4426950408889634f;
    if (wave < 8) {
#pragma unroll
      for (int m = 0; m < 6; ++m) {
        const int trow = (mbase + m) * 16 + c;
        const int tok = tok0 + trow;
        const float rg = rstd[m] * NL2E, ng = nmr[m] * NL2E;
        const f32x2 rg2 = {rg, rg}, ng2 = {ng, ng}, rs2 = {rstd[m], rstd[m]}, nm2 = {nmr[m], nmr[m]};
        unsigned pkv[4];
#pragma unroll
        for (int n = 0; n < 2; ++n)
#pragma unroll
          for (int h = 0; h < 2; ++h) {
            const f32x2 ag = {acc[m][n][2 * h], acc[m][n][2 * h + 1]}, au = {acc[m][n + 2][2 * h], acc[m][n + 2][2 * h + 1]};
            const f32x2 cg = {cs[n][2 * h], cs[n][2 * h + 1]}, cu = {cs[n + 2][2 * h], cs[n + 2][2 * h + 1]};
            const f32x2 eg = __builtin_elementwise_fma(ag, rg2, ng2 * cg);
            const f32x2 uu = __builtin_elementwise_fma(au, rs2, nm2 * cu);
            f32x2 d = {__builtin_amdgcn_exp2f(eg[0]), __builtin_amdgcn_exp2f(eg[1])};
            d = d + f32x2{1.0f, 1.0f};
            const f32x2 hv = uu * f32x2{__builtin_amdgcn_rcpf(d[0]), __builtin_amdgcn_rcpf(d[1])};
            pkv[2 * n + h] = pack2(hv[0], hv[1]);
          }
        if (trow >= 1 && tok < S_)
          st_wt((u32x4*)(a2 + (size_t)(((tok >> 6) * NKS2 + ks) * 4 + ((tok >> 4) & 3)) * 1024 + (16 * q + (tok & 15)) * 16),
                u32x4{pkv[0], pkv[1], pkv[2], pkv[3]});
      }
    } else {
      retile_w2(ws, job, wq, 4, lane);
    }
  } else {
    if (wave < 8) {
#pragma unroll
      for (int m = 0; m < 5; ++m) {
        const int trow = (mbase + m) * 16 + c;
        const f32x2 rs2 = {rstd[m], rstd[m]}, nm2 = {nmr[m], nmr[m]};
#pragma unroll
        for (int n = 0; n < 4; ++n) {
          const f32x2 y0 = __builtin_elementwise_fma(f32x2{acc[m][n][0], acc[m][n][1]}, rs2, nm2 * f32x2{cs[n][0], cs[n][1]});
          const f32x2 y1 = __builtin_elementwise_fma(f32x2{acc[m][n][2], acc[m][n][3]}, rs2, nm2 * f32x2{cs[n][2], cs[n][3]});
          if (n < 3 || wq < 3)
            *LDSP(u32x2, trow * QROW + (ntile[n] * 16 + 4 * q) * 2) = u32x2{pack2(y0[0], y0[1]), pack2(y1[0], y1[1])};
        }
      }
    }
    float bias[8];
#pragma unroll
    for (int j = 0; j < 8; ++j) {
      const int diff = 16 + c - (16 * (j >> 2) + 4 * q + (j & 3));
      bias[j] = (diff >= 0 && diff <= 14 && (diff & 1) == 0) ? 0.f : -3.0e38f;
    }
    const float sl2 = 0.22360679774997896f * 1.4426950408889634f;
    const int dofs = q < 2 ? 16 * q : 32;
    const unsigned qk_l = c * QROW + dofs;
    const unsigned q0_l = q == 3 ? LDS_ZERO : qk_l + 16 * QROW, q0_m = q == 3 ? 0u : 1u;
    const unsigned q1_l = q >= 2 ? LDS_ZERO : qk_l + 16 * QROW + 8, q1_m = q >= 2 ? 0u : 1u;
    const unsigned v_l = (4 * q + (c >> 2)) * QROW + 320 + 8 * (c & 3);
    if (tid == 512) *LDSP(u32x2, LDS_ZERO) = u32x2{0u, 0u};
    wait_lgkm0();
    __builtin_amdgcn_s_barrier();
#pragma unroll 1
    for (int unit = wave; unit < 36; unit += 12) {
      const int mq = 1 + (unit >> 2), hh = unit & 3;
      const unsigned ub = 16 * (mq - 1) * QROW + 40 * hh;
      const u32x2 q0 = *LDSP(u32x2, q0_l + ub * q0_m), q1 = *LDSP(u32x2, q1_l + ub * q1_m);
      const u32x2 k00 = *LDSP(u32x2, ub + qk_l + 160), k01 = *LDSP(u32x2, ub + qk_l + 168);
      const u32x2 k10 = *LDSP(u32x2, ub + qk_l + 16 * QROW + 160), k11 = *LDSP(u32x2, ub + qk_l + 16 * QROW + 168);
      const bf16x8 vf0 = tr_frag(ub + v_l, ub + v_l + 16 * QROW);
      const bf16x8 vf1 = tr_frag(ub + v_l + 32, ub + v_l + 32 + 16 * QROW);
      const bf16x8 qf = __builtin_bit_cast(bf16x8, u32x4{q0[0], q0[1], q1[0], q1[1]});
      const f32x4 z4 = {0.f, 0.f, 0.f, 0.f};
      const f32x4 s0 = __builtin_amdgcn_mfma_f32_16x16x32_bf16(__builtin_bit_cast(bf16x8, u32x4{k00[0], k00[1], k01[0], k01[1]}), qf, z4, 0, 0, 0);
      const f32x4 s1 = __builtin_amdgcn_mfma_f32_16x16x32_bf16(__builtin_bit_cast(bf16x8, u32x4{k10[0], k10[1], k11[0], k11[1]}), qf, z4, 0, 0, 0);
      float lg[8];
      const float sl0 = (tok0 + 16 * (mq - 1) < 0) ? 0.f : sl2;
      const float b0 = (tok0 + 16 * (mq - 1) < 0) ? -3.0e38f : 0.f;
#pragma unroll
      for (int j = 0; j < 4; ++j) {
        lg[j] = __builtin_fmaf(s0[j], sl0, fminf(bias[j], b0));
        lg[4 + j] = __builtin_fmaf(s1[j], sl2, bias[4 + j]);
      }
      float mx = fmaxf(fmaxf(fmaxf(lg[0], lg[1]), fmaxf(lg[2], lg[3])), fmaxf(fmaxf(lg[4], lg[5]), fmaxf(lg[6], lg[7])));
      mx = red_max4(mx);
      float sum = 0.f;
#pragma unroll
      for (int j = 0; j < 8; ++j) { lg[j] = __builtin_amdgcn_exp2f(lg[j] - mx); sum += lg[j]; }
      const float inv = __builtin_amdgcn_rcpf(red_sum4(sum));
      const f32x2 inv2 = {inv, inv};
      const f32x2 p0 = f32x2{lg[0], lg[1]} * inv2, p1 = f32x2{lg[2], lg[3]} * inv2;
      const f32x2 p2 = f32x2{lg[4], lg[5]} * inv2, p3 = f32x2{lg[6], lg[7]} * inv2;
      const bf16x8 pf = __builtin_bit_cast(bf16x8, u32x4{pack2(p0[0], p0[1]), pack2(p1[0], p1[1]), pack2(p2[0], p2[1]), pack2(p3[0], p3[1])});
      const f32x4 o0 = __builtin_amdgcn_mfma_f32_16x16x32_bf16(vf0, pf, z4, 0, 0, 0);
      const f32x4 o1 = __builtin_amdgcn_mfma_f32_16x16x32_bf16(vf1, pf, z4, 0, 0, 0);
      const int tokb = tok0 + 16 * mq;
      if (tokb < S_) {
        char* ob = a2 + (size_t)((tokb >> 6) * NKS2 * 4 + ((tokb >> 4) & 3)) * 1024 + c * 16;
        const int col0 = 80 * hg + 20 * hh + 4 * q;
        *(u32x2*)(ob + (col0 >> 5) * 4096 + ((col0 >> 2) & 3) * 256 + ((col0 >> 4) & 1) * 8) = u32x2{pack2(o0[0], o0[1]), pack2(o0[2], o0[3])};
        const int col1 = 80 * hg + 20 * hh + 16;
        if (q == 0)
          *(u32x2*)(ob + (col1 >> 5) * 4096 + ((col1 >> 2) & 3) * 256 + ((col1 >> 4) & 1) * 8) = u32x2{pack2(o1[0], o1[1]), pack2(o1[2], o1[3])};
      }
    }
  }
}

constexpr int K2_DEPTH = 6;

__global__ __launch_bounds__(256, 1) void k2_kernel(const __hip_bfloat16* __restrict__ x,
                                                    char* __restrict__ ws,
                                                    __hip_bfloat16* __restrict__ out) {
  const int tid = threadIdx.x, lane = tid & 63;
  const int wave = __builtin_amdgcn_readfirstlane(tid >> 6);
  const int q = lane >> 4, c = lane & 15;
  const int xcd = blockIdx.x & 7, idx = blockIdx.x >> 3;
  const int rb = (xcd >> 1) * 8 + (idx >> 2), cb = (xcd & 1) * 4 + (idx & 3);

  const char* ap = ws + WS_A2 + (size_t)(rb * NKS2 + 17 * wave) * 4096 + lane * 16;
  const char* wp = ws + WS_W2 + (size_t)(cb * NKS2 + 17 * wave) * 5120 + lane * 16;

  bf16x8 fa[K2_DEPTH][4], fw[K2_DEPTH][5];
  f32x4 acc[4][5];
#pragma unroll
  for (int m = 0; m < 4; ++m)
#pragma unroll
    for (int n = 0; n < 5; ++n) acc[m][n] = f32x4{0.f, 0.f, 0.f, 0.f};

  auto ld = [&](int i) {
    const int s = i % K2_DEPTH;
    const int kk = i;
#pragma unroll
    for (int m = 0; m < 4; ++m) fa[s][m] = *(const bf16x8*)(ap + kk * 4096 + m * 1024);
#pragma unroll
    for (int n = 0; n < 5; ++n) fw[s][n] = *(const bf16x8*)(wp + kk * 5120 + n * 1024);
  };
#pragma unroll
  for (int i = 0; i < K2_DEPTH; ++i) ld(i);
  const int tok = rb * 64 + wave * 16 + c;
  const size_t obase = (size_t)tok * D_ + cb * 80;
  const u32x4 x0 = *(const u32x4*)(x + obase + 8 * q);
  const u32x4 x1 = *(const u32x4*)(x + obase + 32 + 8 * q);
  const u32x2 x2 = *(const u32x2*)(x + obase + 64 + 4 * q);
  __builtin_amdgcn_sched_barrier(0);
#pragma unroll
  for (int i = 0; i < 17; ++i) {
    const int s = i % K2_DEPTH;
#pragma unroll
    for (int m = 0; m < 4; ++m)
#pragma unroll
      for (int n = 0; n < 5; ++n)
        acc[m][n] = __builtin_amdgcn_mfma_f32_16x16x32_bf16(fw[s][n], fa[s][m], acc[m][n], 0, 0, 0);
    __builtin_amdgcn_sched_barrier(0);
    if (i + K2_DEPTH < 17) ld(i + K2_DEPTH);
    __builtin_amdgcn_sched_barrier(0);
  }

#pragma unroll
  for (int m = 0; m < 4; ++m) {
    if (m != wave) {
      const int si = wave < m ? wave : wave - 1;
#pragma unroll
      for (int n = 0; n < 5; ++n) *LDSP(f32x4, ((m * 3 + si) * 5 + n) * 1024 + lane * 16) = acc[m][n];
    }
  }
  __syncthreads();
  f32x4 r[5];
#pragma unroll
  for (int n = 0; n < 5; ++n) {
    r[n] = wave == 0 ? acc[0][n] : (wave == 1 ? acc[1][n] : (wave == 2 ? acc[2][n] : acc[3][n]));
#pragma unroll
    for (int si = 0; si < 3; ++si) r[n] += *LDSP(f32x4, ((wave * 3 + si) * 5 + n) * 1024 + lane * 16);
  }
  auto lo = [](unsigned v) { return __builtin_bit_cast(float, v << 16); };
  auto hi = [](unsigned v) { return __builtin_bit_cast(float, v & 0xFFFF0000u); };
  u32x4 o0 = {pack2(r[0][0] + lo(x0[0]), r[0][1] + hi(x0[0])), pack2(r[0][2] + lo(x0[1]), r[0][3] + hi(x0[1])),
              pack2(r[1][0] + lo(x0[2]), r[1][1] + hi(x0[2])), pack2(r[1][2] + lo(x0[3]), r[1][3] + hi(x0[3]))};
  u32x4 o1 = {pack2(r[2][0] + lo(x1[0]), r[2][1] + hi(x1[0])), pack2(r[2][2] + lo(x1[1]), r[2][3] + hi(x1[1])),
              pack2(r[3][0] + lo(x1[2]), r[3][1] + hi(x1[2])), pack2(r[3][2] + lo(x1[3]), r[3][3] + hi(x1[3]))};
  u32x2 o2 = {pack2(r[4][0] + lo(x2[0]), r[4][1] + hi(x2[0])), pack2(r[4][2] + lo(x2[1]), r[4][3] + hi(x2[1]))};
  *(u32x4*)(out + obase + 8 * q) = o0;
  *(u32x4*)(out + obase + 32 + 8 * q) = o1;
  *(u32x2*)(out + obase + 64 + 4 * q) = o2;
}

extern "C" void kernel_launch(void* const* d_in, const int* in_sizes, int n_in,
                              void* d_out, int out_size, void* d_ws, size_t ws_size,
                              hipStream_t stream) {
  const __hip_bfloat16* x = (const __hip_bfloat16*)d_in[0];
  const __hip_bfloat16* wqkv = (const __hip_bfloat16*)d_in[1];
  const __hip_bfloat16* wo = (const __hip_bfloat16*)d_in[2];
  const __hip_bfloat16* wg = (const __hip_bfloat16*)d_in[4];
  const __hip_bfloat16* wu = (const __hip_bfloat16*)d_in[5];
  const __hip_bfloat16* wd = (const __hip_bfloat16*)d_in[6];
  (void)hipFuncSetAttribute((const void*)k1_kernel, hipFuncAttributeMaxDynamicSharedMemorySize, LDS_K1);
  k1_kernel<<<256, 768, LDS_K1, stream>>>(x, wqkv, wo, wg, wu, wd, (char*)d_ws);
  k2_kernel<<<256, 256, 61440, stream>>>(x, (char*)d_ws, (__hip_bfloat16*)d_out);
}
```
